```python
import torch

def _softmax(x, axis=-1):
    return torch.softmax(x, dim=axis)
def _gelu(x):
    return torch.nn.functional.gelu(x, approximate="tanh")
def _silu(x):
    return torch.nn.functional.silu(x)
def _sigmoid(x):
    return torch.sigmoid(x)
def _layernorm(x, axis=-1, eps=1e-5):
    m = x.mean(dim=axis, keepdim=True)
    v = x.var(dim=axis, keepdim=True, unbiased=False)
    return (x - m) / torch.sqrt(v + eps)
def _scatter_add(tgt, idx, src, axis=0):
    return tgt.clone().index_add_(axis, idx, src)
def _segment_sum(src, seg, n):
    out = torch.zeros((n,) + src.shape[1:], dtype=src.dtype)
    return out.index_add_(0, seg, src)
def _pad(x, pw):
    flat = tuple(v for pair in reversed(pw) for v in pair)
    return torch.nn.functional.pad(x, flat)

kernel_name = 'standalone__weight_only_int_squeezellm__prompt'

def setup_inputs():

    _w = (lambda a: (lambda t: t.to(torch.bfloat16) if t.is_floating_point() else t)(torch.as_tensor(a)))
    B, S, D, F = 1, 2048, 1024, 3584
    bits = 8 if (2 <= 8 <= 4) else 4
    L = 2 ** bits
    qw = torch.randint(0, L, (D, F)).to(torch.int64)
    lut = (lambda *s: torch.randn(*s, dtype=torch.bfloat16) * 0.05)(L, F)
    return {
        "x": _w((lambda *s: torch.randn(*s, dtype=torch.bfloat16) * 0.05)(B, S, D)),
        "qw": _w(qw),
        "lut": _w(lut),
    }

def reference(x, qw, lut):
    w = (lambda x, i, axis: torch.take_along_dim(x, i, dim=axis))(lut, qw, axis=0)
    w = (lambda x, dt: x.to(dt))(w, x.dtype)
    return torch.einsum('bsd,df->bsf', x, w)
```

```cpp
#include <hip/hip_runtime.h>
#include <hip/hip_bf16.h>

using bf16x8 = __attribute__((ext_vector_type(8))) short;
using f32x4 = __attribute__((ext_vector_type(4))) float;
using i32x4 = __attribute__((ext_vector_type(4))) int;
using u32x4 = __attribute__((ext_vector_type(4))) unsigned;
using u32x2 = __attribute__((ext_vector_type(2))) unsigned;
typedef unsigned short u16;
typedef unsigned long long u64;
#define XSE 0
#define HOIST 1
#define EB 2
#define CR 1
#define QP3 ""
#define ALN 1
#define BB 1
#define OWNN 3
#define PG2 1
#define CTL 0
#define HORD O_L DLY(9) O_Q

constexpr int M_ = 2048, K_ = 1024, N_ = 3584;
constexpr int BM = 128, BN = 224, BK = 64, NT = K_ / BK;
constexpr int W_OFF = 49152;
constexpr int PL = BN * 16;
constexpr int WB = 8 * PL;
constexpr int ST_OFF = W_OFF + 3 * WB;
constexpr int SMEM_B = 163840;
constexpr int PSTR = NT * WB + 4096;
constexpr size_t WG_OFF = 1u << 20;

#define QPOL "nt"
#define OPOL "sc1"
#define SHMUL 0
#define RCMUL 0
#define FLIP 0
#define DINV 0
#define QP4 "nt"
#define XM 0
#define E3R 0
#define QM 0
#define XB 0
#define QPOL3 QPOL2
#define EPV (EB ? 3 : 7)
#define EPW (1u << EPV)
#define NLOC 1
#define QPOL2 "nt"
#define MAPX 0
#define WPOLL "sc1"
#define OTHER 1
#define PSLEEP 1
#define GMASK 1
#define GSH 0
#define HLOW 1
#define EPE 0
#define HPOS 1
#define XS_A 0
#define XS_B 5
#define XJD 3
#define HSL 0
#define OWNF 0
#define PGW 0
#define DL 0
#define XTL 0
#define PSL2 8
#define LOCK2 1
#define GM2 1
#define NALL 1
#define KALIGN __attribute__((aligned(4096)))
#define CSPLIT 0
#define XPOL ""
#define XTI bid
#define PAL ".p2align 12\n"
#define SB() __builtin_amdgcn_sched_barrier(0)
#define DSREAD(dst, vaddr, imm) \
    asm volatile("ds_read_b128 %0, %1 offset:%2" : "=v"(dst) : "v"(vaddr), "i"(imm) : "memory")
#define PUTREC
#define TS(i)
#define CLK(tt)

template <int N> __device__ __forceinline__ void wait_vm() { asm volatile("s_waitcnt vmcnt(%0)" ::"i"(N) : "memory"); }
template <int N> __device__ __forceinline__ void wait_lgkm() { asm volatile("s_waitcnt lgkmcnt(%0)" ::"i"(N) : "memory"); }

__global__ __launch_bounds__(512, 2) KALIGN void fused_kernel(const u16* __restrict__ A, const int* __restrict__ qw,
                                                       const u16* __restrict__ lut, u16* __restrict__ C, char* ws) {
    __shared__ __attribute__((aligned(16))) char smem[SMEM_B];
    const unsigned bid = blockIdx.x, t = threadIdx.x;
    unsigned tv = ((bid & ~7u) + (t >> 6)) << 7;
    u64 pc0;
    {
        unsigned ce;
        asm volatile("s_getpc_b64 %1\nsq_k_beg:\n\ts_mov_b32 %2, sq_k_end0-sq_k_beg\n\ts_max_u32 %2, sq_k_end1-sq_k_beg, %2\n\tv_min_u32 %0, %2, %0"
                     : "+v"(tv), "=&s"(pc0), "=&s"(ce) :: "memory", "scc");
    }
#define O_C asm volatile("s_mov_b64 exec, 1\n\tglobal_load_dword %0, %0, %1\n\ts_mov_b64 exec, -1" : "+v"(tv) : "s"(pc0) : "memory");
    O_C
    SB();
    const int pn = ((bid & 6) << 1) + (bid >> 6), pm = (((bid & 1) ^ FLIP) << 3) | ((bid >> 3) & 7);
    const int bcol = pn * BN;
    const int so = pm & ~1;
    const int lane = t & 63, jc = lane < 56 ? lane : lane - 8;
    u32x2 lv[4], ep = {0u, 0u};
    const int wid = __builtin_amdgcn_readfirstlane(t >> 6);
    i32x4 q[4], q2[4], q3[4], q4[4];
    unsigned qv[4], eo;
    {
        qv[0] = ((t >> 6) ^ ((bid >> 1) & 4)) * (32u * N_) + jc * 16 + (unsigned)(so * (BK * N_) + bcol) * 4u;
#pragma unroll
        for (int i = 1; i < 4; ++i) qv[i] = qv[0] + i * (4u * N_);
        unsigned lo = (t >> 6) * ((EB ? 8u : 4u) * N_) + jc * 8 + bcol * 2u;
        unsigned lo2 = lo + 2u * N_, lo3 = lo + 4u * N_, lo4 = lo + 6u * N_;
        const unsigned e0 = 65536u + bid * 128u, e1 = (unsigned)WG_OFF + pn * PSTR + NT * WB + pm * 128;
        eo = e0 + (wid & 1) * (e1 - e0) + (((unsigned)(wid ^ EPV) - 1u) >> 31) * (pn * 2048u - e1);
        if (EB) asm volatile("" : "+v"(lo3), "+v"(lo4));
        asm volatile("ds_write_b64 %0, %1" :: "v"((unsigned)(size_t)(__attribute__((address_space(3))) char*)smem + ST_OFF + 8192 + 64), "v"(ep) : "memory");
        asm volatile("" : "+v"(lo), "+v"(lo2), "+v"(eo), "+v"(ep), "+v"(qv[0]), "+v"(qv[1]), "+v"(qv[2]), "+v"(qv[3]));
        SB();
#define O_L asm volatile("s_cmp_gt_u32 %9, 3\n\ts_cbranch_scc1 1f\n\tglobal_load_dwordx2 %0, %4, %8\n\tglobal_load_dwordx2 %1, %5, %8\n\tglobal_load_dwordx2 %2, %6, %8\n\tglobal_load_dwordx2 %3, %7, %8\n1:" \
                     : "=&v"(lv[0]), "=&v"(lv[1]), "=&v"(lv[2]), "=&v"(lv[3]) : "v"(lo), "v"(lo2), "v"(lo3), "v"(lo4), "s"(lut), "s"(wid), "s"(ws), "s"(A), "s"(C) : "memory", "scc");
#define O_E asm volatile("s_mov_b64 exec, 1\n\ts_cmp_eq_u32 %4, %5\n\ts_cbranch_scc1 1f\n\tglobal_load_dword %1, %2, %3\n\ts_branch 2f\n1:\n\t" \
                     "global_atomic_or_x2 %0, %2, %0, %3 sc0 sc1\n2:\n\ts_mov_b64 exec, -1" : "+v"(ep), "+v"(tv) : "v"(eo), "s"(ws), "s"(wid), "i"(EPV) : "memory", "scc");
#define DLY(n) asm volatile("s_bitcmp1_b32 %0, %2\n\ts_cbranch_scc0 1f\n\ts_sleep %1\n1:" :: "s"(XM & 2 ? bid ^ (bid >> 2) : bid ^ DINV), "i"((n) % 100), "i"((n) < 100 ? 5 : (n) / 100 - 1) : "memory", "scc");
#define Q4M(POL) "s_mov_b32 exec_hi, 0xffffff\n\tglobal_load_dwordx4 %0, %4, %8 " POL "\n\tglobal_load_dwordx4 %1, %5, %8 " POL "\n\tglobal_load_dwordx4 %2, %6, %8 " POL "\n\tglobal_load_dwordx4 %3, %7, %8 " POL "\n\ts_mov_b32 exec_hi, -1"
#define Q4L(q, base, POL) asm volatile(Q4M(POL) : "=&v"(q[0]), "=&v"(q[1]), "=&v"(q[2]), "=&v"(q[3]) : "v"(qv[0]), "v"(qv[1]), "v"(qv[2]), "v"(qv[3]), "s"(base) : "memory");
#define O_Q _Pragma("unroll") for (int i = 0; i < 4; ++i) asm volatile("global_load_dwordx4 %0, %1, %2 " QPOL : "=v"(q[i]) : "v"(qv[i]), "s"(qw) : "memory");
#define O_R _Pragma("unroll") for (int i = 0; i < 4; ++i) asm volatile("global_load_dwordx4 %0, %1, %2 " QPOL : "=v"(q2[i]) : "v"(qv[i]), "s"(qw + 4 * N_) : "memory");
        HORD
        SB();
    }
#define QLOAD(q, base) _Pragma("unroll") for (int i = 0; i < 4; ++i) \
        asm volatile("global_load_dwordx4 %0, %1, %2 " QPOL : "=v"(q[i]) : "v"(qv[i]), "s"(base) : "memory");
    char* hand = ws + pn * 2048;
    const unsigned zero = 0;
#define EPWT asm volatile("s_mov_b64 exec, 1\n\ts_cmp_eq_u32 %5, 7\n\ts_cbranch_scc1 1f\n\tglobal_load_dword %1, %2, %4\n\ts_branch 2f\n1:\n\t" \
                 "global_atomic_or_x2 %0, %2, %0, %3 sc0 sc1\n2:\n\ts_mov_b64 exec, -1" : "+v"(ep), "+v"(tv) : "v"(zero), "s"(hand), \
        "s"(ws + (65536u + bid * 128u + (wid & 1) * ((unsigned)WG_OFF + pn * PSTR + NT * WB + pm * 128 - 65536u - bid * 128u))), "s"(wid) : "memory", "scc");
    const int brow = pm * BM, odd = pn & 1;
    const unsigned e4 = 0x6420u ^ ((((((pm >> GSH) & GMASK) ^ (pn & XM)) * 2) | ((pm & 8) ^ 8)) * 0x1111u);
    const unsigned klo = (1u << (2 * (pm & 6))) - 1u;
    const unsigned e3a = (((0x6420u & klo) | (0x0642u & ~klo)) & 0xFFFu) | ((pm & 8) * 0x111u);
    const unsigned e3 = E3R ? ((e3a * 0x1001u) >> (4 * (pn & 3))) & 0xFFFu : e3a;
    const u64 kte = ((u64)((e4 | 0x1111u) | (((e4 ^ 0x8888u) | 0x1111u) << 16)) << 28) | (e4 | (e3 << 16));
    const unsigned e4b = 0x7531u ^ ((((pm & GM2) * 2) | ((pm & 8) ^ 8)) * 0x1111u);
    const u64 kto = ((u64)(e4b | ((e3 | 0x111u) << 16)) << 32) | ((BB == 2 ? (e4 & 15u) | ((pm | 1) << 4) | ((e4 >> 4) << 8) : (pm | 1) | (e4 << 4)) | ((E3R == 2 ? e3a : e3) << 20));
    const u64 ktw = ((u64)(OWNF == 5 ? (e4 | 0x1111u) | ((e3 | 0x111u) << 16) : (e3 | 0x111u) | ((e4 | 0x1111u) << 12)) << 32) | ((pm | 1) | (e3 << 4) | (e4 << 16));
    const u64 ktab0 = OWNF >= 4 ? ((pm & 8) ? ktw : kto) : OWNF == 3 ? (kto & 0xFFFFFFFFFF000FFFull) | ((e3 & 15u) << 12) | ((e4 >> 8) << 16) : OWNF == 0 ? kto : ((u64)(OWNF == 1 ? (e4 | 0x1111u) | ((e3 | 0x111u) << 16) : (e3 | 0x111u) | ((e4 | 0x1111u) << 12)) << 32) | ((pm | 1) | (e3 << 4) | (e4 << 16));
#define KT(n) ((n) == 0 ? so : (int)((unsigned)(ktab0 >> (4 * (((n) - 1) & 15))) & 15u))
    u64 sd = 0;
    const int ap = wid ^ ((pm & 1) << 2), dp = XB ? 0 : wid - ap;
#define XS1(n) asm volatile("s_load_dwordx2 %0, %1, %2 offset:60" : "+&s"(sd) : "s"(A + (size_t)(brow + pn * 8 + wid) * K_), "s"(KT(n) * 128) : "memory");
#define XJK(k_) asm volatile("s_load_dwordx2 %0, %1, %2 offset:60\n\ts_load_dwordx2 %0, %1, %2 offset:2108" : "+&s"(sd) : "s"(A + (size_t)(brow + pn * 8 + (wid & 3) * 2) * K_), "s"((k_) * 128) : "memory");
#define PGS { u64 pc_; asm volatile("s_getpc_b64 %1\n\ts_load_dwordx2 %0, %1, 0x1000\n\ts_load_dwordx2 %0, %1, 0x2000\n\ts_load_dwordx2 %0, %1, 0x3000" PGX : "+&s"(sd), "=&s"(pc_) :: "memory"); }
#define PGX
#define PGC asm volatile("s_cmp_eq_u32 %0, 4\n\ts_cbranch_scc0 1f\n\ts_call_b64 s[60:61], sq_pg1\n1:\n\ts_cmp_eq_u32 %0, 5\n\ts_cbranch_scc0 2f\n\ts_call_b64 s[60:61], sq_pg2\n2:\n\t" \
        "s_cmp_eq_u32 %0, 6\n\ts_cbranch_scc0 3f\n\ts_call_b64 s[60:61], sq_pg3\n3:\n\ts_cmp_eq_u32 %0, 7\n\ts_cbranch_scc0 4f\n\ts_call_b64 s[60:61], sq_pg4\n4:" :: "s"(wid ^ PGW) : "s60", "s61", "scc");
#define PGH2 asm volatile("s_branch 1f\n" PAL "sq_pg1:\n\ts_setpc_b64 s[60:61]\n1:");
#define PG1 ""
#define PGH asm volatile("s_branch 1f\n" PAL "sq_pg4:\n\ts_setpc_b64 s[60:61]\n1:");
#define XSTART { if (XS_A <= 0 && XS_B >= 0) XS1(0) if (XS_A <= 1 && XS_B >= 1) XS1(1) if (XS_A <= 2 && XS_B >= 2) XS1(2) if (XS_A <= 3 && XS_B >= 3) XS1(3) if (XS_B >= 4) XS1(4) if (XS_B >= 5) XS1(5) if (XS_B >= 6) XS1(6) if (XS_B >= 7) XS1(7) if (XS_B >= 8) XS1(8) }
    SB();
#define CRO(df) ((df) ^ (CR ? (pm & 1) * 4 : 0))
#define QLOADX(q, df) _Pragma("unroll") for (int i = 0; i < 4; ++i) { \
        if ((df) == BK) asm volatile("global_load_dwordx4 %0, %1, %2 " QP3 : "=v"(q[i]) : "v"(qv[i]), "s"(qw + (ptrdiff_t)(CRO(df) + dp * 8) * N_) : "memory"); \
        else asm volatile("global_load_dwordx4 %0, %1, %2 " QP4 : "=v"(q[i]) : "v"(qv[i]), "s"(qw + (ptrdiff_t)(CRO(df) + dp * 8) * N_) : "memory"); }
#define QL1(R, q, i, df, POL) asm volatile("global_load_dwordx4 %0, %1, %2 " POL : "={" R "}"(q) : "v"(qv[i]), "s"(qw + (ptrdiff_t)(CRO(df) + dp * 8) * N_) : "memory");
#define QLOAD3 QL1("v[216:219]", q3[0], 0, BK, QP3) QL1("v[220:223]", q3[1], 1, BK, QP3) QL1("v[224:227]", q3[2], 2, BK, QP3) QL1("v[228:231]", q3[3], 3, BK, QP3)
#define QLOAD4 QL1("v[232:235]", q4[0], 0, BK + 4, QP4) QL1("v[236:239]", q4[1], 1, BK + 4, QP4) QL1("v[240:243]", q4[2], 2, BK + 4, QP4) QL1("v[244:247]", q4[3], 3, BK + 4, QP4)
    const unsigned lds0 = (unsigned)(size_t)(__attribute__((address_space(3))) char*)smem;
    TS(0)
    const bool helper = HLOW ? wid < 4 : wid >= 4;
    const int hw = wid & 3;
    const unsigned l16 = lane * 16;
    const unsigned lbase = lds0 + ST_OFF + lane * 4;
    unsigned woff[4];
    {
        const int cg = (jc >> 3) - odd, jj = jc & 7;
        const int m3 = -(int)((unsigned)cg > 5u);
        const int rho = ((96 + 2 * (jj & 3)) & m3) | (((cg & 6) * 16 + jj) & ~m3), sd = ((jj >> 2) & m3) | (cg & 1 & ~m3);
        woff[0] = lds0 + W_OFF + ap * PL + sd * (PL / 2) + rho * 16 + (QM ? ((lane + 8) & 64) << 14 : 0);
        woff[1] = woff[0] + 256 - (m3 & 128);
        woff[2] = woff[0] + 128 - (m3 & 112);
        woff[3] = woff[0] + 384 - (m3 & 240);
    }
#define DEQUANTH(q, io, H, FIN) { \
        asm volatile("" : "+v"(q[0]), "+v"(q[1]), "+v"(q[2]), "+v"(q[3])); \
        unsigned wl[4][4]; \
        _Pragma("unroll") for (int c = 0; c < 4; ++c) { \
            _Pragma("unroll") for (int i = 0; i < 4; ++i) wl[c][i] = lbase + ((unsigned)q[i][c] << 9); \
            _Pragma("unroll") for (int i = 0; i < 4; ++i) \
                asm volatile("ds_read_b32 %0, %0 offset:%1" : "+v"(wl[c][i]) : "i"((c >> 1) * 256) : "memory"); \
            if (HPOS == 0) { H(c) } \
            SB(); } \
        _Pragma("unroll") for (int c = 0; c < 4; ++c) { \
            if (HPOS == 1) { H(c) } \
            if (c == 0) wait_lgkm<12>(); if (c == 1) wait_lgkm<9>(); if (c == 2) wait_lgkm<6>(); if (c == 3) wait_lgkm<3>(); \
            asm volatile("" : "+v"(wl[c][0]), "+v"(wl[c][1]), "+v"(wl[c][2]), "+v"(wl[c][3])); \
            const u32x2 w = {__builtin_amdgcn_perm(wl[c][1], wl[c][0], (c & 1) ? 0x07060302u : 0x05040100u), \
                             __builtin_amdgcn_perm(wl[c][3], wl[c][2], (c & 1) ? 0x07060302u : 0x05040100u)}; \
            asm volatile("ds_write_b64 %0, %1 offset:%2" :: "v"(woff[c]), "v"(w), "i"(io) : "memory"); \
            if (HPOS == 2) { H(c) } \
            SB(); \
        } if (FIN) wait_lgkm<0>(); }
#define HNONE(c)
#define QX1(q, i, df) asm volatile("global_load_dwordx4 %0, %1, %2 " QPOL2 : "=v"(q[i]) : "v"(qv[i]), "s"(qw + (ptrdiff_t)((df) + dp * 8) * N_) : "memory");
#define HQ3(c) QX1(q3, c, BK)
#define HQ4(c) QX1(q4, c, BK + 4)
#define HQ6(c) QX1(q, c, BK + 36)
#define HQ3p(c) if (c == 0) QL1("v[216:219]", q3[0], 0, BK, QP3) if (c == 1) QL1("v[220:223]", q3[1], 1, BK, QP3) if (c == 2) QL1("v[224:227]", q3[2], 2, BK, QP3) if (c == 3) QL1("v[228:231]", q3[3], 3, BK, QP3)
#define HQ4p(c) if (c == 0) QL1("v[232:235]", q4[0], 0, BK + 4, QP4) if (c == 1) QL1("v[236:239]", q4[1], 1, BK + 4, QP4) if (c == 2) QL1("v[240:243]", q4[2], 2, BK + 4, QP4) if (c == 3) QL1("v[244:247]", q4[3], 3, BK + 4, QP4)
#define DEQUANT(q, io) DEQUANTH(q, io, HNONE, 1)
    const u16* Aw = A + (size_t)(brow + 32 * hw) * K_;
    unsigned dv[4];
#pragma unroll
    for (int d = 0; d < 4; ++d) dv[d] = (unsigned)((lane >> 3) * 2048 + (((lane & 7) ^ (4 * (d & 1) + (lane >> 4))) << 4) + d * 15360);
#define XTILEK(k_, bf) XTILEA(k_, hw * 12288 + (bf) * 4096)
#define XTILEA(k_, a_) \
        asm volatile("s_mov_b32 m0, %0\n\ts_nop 0\n\t" \
                     "global_load_lds_dwordx4 %1, %5 " XPOL "\n\tglobal_load_lds_dwordx4 %2, %5 offset:1024 " XPOL "\n\t" \
                     "global_load_lds_dwordx4 %3, %5 offset:2048 " XPOL "\n\tglobal_load_lds_dwordx4 %4, %5 offset:3072 " XPOL \
                     :: "s"(lds0 + (a_)), "v"(dv[0]), "v"(dv[1]), "v"(dv[2]), "v"(dv[3]), "s"(Aw + (k_) * BK) : "memory");
#define XTILE(tt) { int k_ = KT(tt); asm volatile("" : "+s"(k_)); XTILEK(k_, (tt) % 3) }
#define XHALF(tt) asm volatile("s_mov_b32 m0, %0\n\ts_nop 0\n\tglobal_load_lds_dwordx4 %1, %3\n\tglobal_load_lds_dwordx4 %2, %3 offset:1024" \
                     :: "s"(lds0 + hw * 12288 + (tt) * 4096 + (wid >> 2) * 2048), "v"(dv[0]), "v"(dv[1]), "s"(Aw + (wid >> 2) * (16 * K_) + KT(tt) * BK) : "memory");
    char* Wp = ws + WG_OFF + (size_t)pn * PSTR;
#define LUTW4 asm volatile("" : "+v"(lv[0]), "+v"(lv[1]), "+v"(lv[2]), "+v"(lv[3])); \
        asm volatile("ds_write2st64_b32 %0, %1, %2 offset1:1\n\tds_write2st64_b32 %0, %3, %4 offset0:2 offset1:3\n\tds_write2st64_b32 %0, %5, %6 offset0:4 offset1:5\n\tds_write2st64_b32 %0, %7, %8 offset0:6 offset1:7" \
                 :: "v"(lbase + wid * 2048), "v"(lv[0][0]), "v"(lv[0][1]), "v"(lv[1][0]), "v"(lv[1][1]), "v"(lv[2][0]), "v"(lv[2][1]), "v"(lv[3][0]), "v"(lv[3][1]) : "memory");
#define LUTWRITE asm volatile("" : "+v"(lv[0]), "+v"(lv[1])); \
        asm volatile("ds_write2_b32 %0, %1, %2 offset1:64\n\tds_write2_b32 %0, %3, %4 offset0:128 offset1:192" \
                 :: "v"(lbase + wid * 1024), "v"(lv[0][0]), "v"(lv[0][1]), "v"(lv[1][0]), "v"(lv[1][1]) : "memory");
#define SETUP asm volatile("" : "+v"(woff[0]), "+v"(woff[1]), "+v"(woff[2]), "+v"(woff[3]), "+v"(dv[0]), "+v"(dv[1]), "+v"(dv[2]), "+v"(dv[3]));
#define XTOUCH8 asm volatile("s_mov_b64 exec, 0xffff\n\tglobal_load_dword %0, %1, %2\n\ts_mov_b64 exec, -1" : "+v"(tv) : "v"((unsigned)(XTI * 128 + wid * 16 + (lane & 15)) * 128u), "s"(A) : "memory");
#define XTOUCHA asm volatile("s_mov_b64 exec, 0xff\n\tglobal_load_dword %0, %1, %2\n\ts_mov_b64 exec, -1" : "+v"(tv) : "v"((unsigned)(bid * 128 + wid * 16 + ((bid >> 4) & 8 ^ 8) + (lane & 7)) * 128u), "s"(A) : "memory");
#define XTOUCHB asm volatile("s_mov_b64 exec, 0xffff\n\tglobal_load_dword %0, %1, %2\n\ts_mov_b64 exec, -1" : "+v"(tv) : "v"((unsigned)(bid * 128 + hw * 32 + (lane & 8) * 2 + ((bid >> 4) & 8) + (lane & 7)) * 128u), "s"(A) : "memory");
#define XTOUCH4 asm volatile("s_mov_b32 exec_hi, 0\n\tglobal_load_dword %0, %1, %2\n\ts_mov_b32 exec_hi, -1" : "+v"(tv) : "v"((unsigned)(bid * 128 + hw * 32 + (lane & 31)) * 128u), "s"(A) : "memory");
#define WSTOUCH asm volatile("s_mov_b64 exec, %3\n\tglobal_load_dword %0, %1, %2\n\ts_mov_b64 exec, -1" : "+v"(tv) : "v"(zero), \
        "s"(ws + (65536u + bid * 128u + (wid & 1) * ((unsigned)WG_OFF + pn * PSTR + NT * WB + pm * 128 - 65536u - bid * 128u))), "s"((u64)((3u >> wid) & 1u)) : "memory");
#define EPLOAD asm volatile("s_lshr_b32 exec_lo, %4, %3\n\ts_and_b64 exec, exec, 1\n\tglobal_atomic_or_x2 %0, %1, %0, %2 sc0 sc1\n\ts_mov_b64 exec, -1" \
                 : "+v"(ep) : "v"(zero), "s"(hand), "s"(wid), "i"(EPW) : "memory", "scc");
#define EPPUT asm volatile("s_lshr_b32 exec_lo, %3, %2\n\ts_and_b64 exec, exec, 1\n\tds_write_b64 %0, %1\n\ts_mov_b64 exec, -1" :: "v"(lds0 + ST_OFF + 8192), "v"(ep), "s"(wid), "i"(EPW) : "memory", "scc");
#define GETTAG asm volatile("ds_read_b64 %0, %1\n\ts_waitcnt lgkmcnt(0)" : "=v"(ep) : "v"(lds0 + ST_OFF + 8192) : "memory"); \
        const u64 tag = (((u64)(unsigned)__builtin_amdgcn_readfirstlane(ep[1]) << 32) | (unsigned)__builtin_amdgcn_readfirstlane(ep[0])) + 0x9E3779B97F4A7C15ull; \
        const unsigned tlo = (unsigned)tag, thi = (unsigned)(tag >> 32);
#define PUTWORD(off) { const u32x2 tg = {tlo, thi}; const unsigned fo = (off); \
        asm volatile("s_mov_b64 exec, 1\n\tglobal_atomic_swap_x2 %0, %1, %2 sc1\n\ts_mov_b64 exec, -1" :: "v"(fo), "v"(tg), "s"(hand) : "memory"); }
#define FWARM asm volatile("s_mov_b64 exec, 1\n\tglobal_atomic_or %0, %1, %2 sc1\n\ts_mov_b64 exec, -1" :: "v"(256u + wid * 128u + so * 8u), "v"(zero), "s"(hand) : "memory");
#define IFODD "s_bitcmp1_b32 %[b], 5\n\ts_cbranch_scc0 9f\n\t"
#define IFEVEN "s_bitcmp1_b32 %[b], 5\n\ts_cbranch_scc1 9f\n\t"
#define PUBP(SKIP, lo_, tl_, pl_) { u32x4 pb[4]; \
        asm volatile(SKIP "ds_read_b128 %0, %4\n\tds_read_b128 %1, %4 offset:1024\n\tds_read_b128 %2, %4 offset:2048\n\tds_read_b128 %3, %4 offset:3072\n\t" \
                     "s_waitcnt lgkmcnt(3)\n\tglobal_store_dwordx4 %5, %0, %6 sc1\n\ts_waitcnt lgkmcnt(2)\n\tglobal_store_dwordx4 %5, %1, %6 offset:1024 sc1\n\t" \
                     "s_waitcnt lgkmcnt(1)\n\tglobal_store_dwordx4 %5, %2, %6 offset:2048 sc1\n\ts_waitcnt lgkmcnt(0)\n\ts_mov_b32 exec_hi, 0\n\t" \
                     "global_store_dwordx4 %5, %3, %6 offset:3072 sc1\n\ts_mov_b32 exec_hi, -1\n\ts_nop 0\n9:" \
                     : "=&v"(pb[0]), "=&v"(pb[1]), "=&v"(pb[2]), "=&v"(pb[3]) \
                     : "v"(lds0 + W_OFF + (lo_) + l16), "v"(l16), "s"(Wp + (tl_) * WB + (pl_) * PL), [b] "s"(bid) : "memory", "scc"); }
#define PUBH(lo_, tl_, pl_, o_, eh_) { u32x4 pb[2]; \
        asm volatile("ds_read_b128 %0, %2 offset:%5\n\tds_read_b128 %1, %2 offset:%5+1024\n\t" \
                     "s_waitcnt lgkmcnt(1)\n\tglobal_store_dwordx4 %3, %0, %4 offset:%5 sc1\n\ts_waitcnt lgkmcnt(0)\n\ts_mov_b32 exec_hi, %6\n\t" \
                     "global_store_dwordx4 %3, %1, %4 offset:%5+1024 sc1\n\ts_mov_b32 exec_hi, -1\n\ts_nop 0" \
                     : "=&v"(pb[0]), "=&v"(pb[1]) \
                     : "v"(lds0 + W_OFF + (lo_) + l16), "v"(l16), "s"(Wp + (tl_) * WB + (pl_) * PL), "i"(o_), "i"(eh_) : "memory", "scc"); }
    u32x4 pd[4];
#define PUBR(lo_) asm volatile("ds_read_b128 %0, %4\n\tds_read_b128 %1, %4 offset:1024\n\tds_read_b128 %2, %4 offset:2048\n\tds_read_b128 %3, %4 offset:3072\n\ts_waitcnt lgkmcnt(0)" \
                     : "=&v"(pd[0]), "=&v"(pd[1]), "=&v"(pd[2]), "=&v"(pd[3]) : "v"(lds0 + W_OFF + (lo_) + l16) : "memory");
#define PUBW(tl_, pl_) asm volatile("global_store_dwordx4 %4, %0, %5 sc1\n\tglobal_store_dwordx4 %4, %1, %5 offset:1024 sc1\n\tglobal_store_dwordx4 %4, %2, %5 offset:2048 sc1\n\t" \
                     "s_mov_b32 exec_hi, 0\n\tglobal_store_dwordx4 %4, %3, %5 offset:3072 sc1\n\ts_mov_b32 exec_hi, -1\n\ts_nop 0" \
                     :: "v"(pd[0]), "v"(pd[1]), "v"(pd[2]), "v"(pd[3]), "v"(l16), "s"(Wp + (tl_) * WB + (pl_) * PL) : "memory");
#define PUBW1(tl_, pl_, i) asm volatile("s_mov_b32 exec_hi, %4\n\tglobal_store_dwordx4 %1, %0, %2 offset:%3 sc1\n\ts_mov_b32 exec_hi, -1\n\ts_nop 0" \
                     :: "v"(pd[i]), "v"(l16), "s"(Wp + (tl_) * WB + (pl_) * PL), "i"((i) * 1024), "i"((i) < 3 ? -1 : 0) : "memory");
#define FLAGIF(SKIP, n, off) { const u32x2 tg = {tlo, thi}; const unsigned fo = (off); \
        asm volatile(SKIP "s_waitcnt vmcnt(%3)\n\ts_mov_b64 exec, 1\n\tglobal_atomic_swap_x2 %0, %1, %2 sc1\n\ts_mov_b64 exec, -1\n9:" \
                     :: "v"(fo), "v"(tg), "s"(hand), "i"(n), [b] "s"(bid) : "memory", "scc"); }
#define WAIT2(SKIP, n, m) asm volatile("s_waitcnt vmcnt(%0)\n\t" SKIP "s_waitcnt vmcnt(%1)\n9:" :: "i"(n), "i"(m), [b] "s"(bid) : "memory", "scc");
    const int pla = hw + 4 * (pm & 1);
    constexpr int SCR = 3 * WB + 8448;
    const unsigned cadr = lds0 + ST_OFF + 8192 + 64, one = 1;
#define CZERO asm volatile("ds_write_b32 %0, %1" :: "v"(cadr), "v"(zero) : "memory");
#define LADD asm volatile("s_mov_b64 exec, 1\n\tds_add_u32 %0, %1 offset:4\n\ts_mov_b64 exec, -1" :: "v"(cadr), "v"(one) : "memory");
#define LWAIT(n) { unsigned c_; asm volatile("1:\n\tds_read_b32 %0, %1 offset:4\n\ts_waitcnt lgkmcnt(0)\n\tv_cmp_gt_u32 vcc, %3, %0\n\ts_nop 1\n\ts_cbranch_vccnz 1b" : "=&v"(c_) : "v"(cadr), "s"(sd), "i"(n) : "memory", "vcc"); }
#define CADD asm volatile("s_waitcnt lgkmcnt(0)\n\ts_mov_b64 exec, 1\n\tds_add_u32 %0, %1\n\ts_mov_b64 exec, -1" :: "v"(cadr), "v"(one) : "memory");
#define CWAIT { unsigned c_; asm volatile("1:\n\tds_read_b32 %0, %1\n\ts_waitcnt lgkmcnt(0)\n\tv_cmp_ne_u32 vcc, 0x48, %0\n\ts_nop 1\n\ts_cbranch_vccnz 1b" : "=&v"(c_) : "v"(cadr) : "memory", "vcc"); }
#define CWAITA { unsigned c_; asm volatile("1:\n\tds_read_b32 %0, %1\n\ts_waitcnt lgkmcnt(0)\n\tv_cmp_lt_u32 vcc, %0, 64\n\ts_nop 1\n\ts_cbranch_vccnz 1b" : "=&v"(c_) : "v"(cadr) : "memory", "vcc"); }
#define XSC(par) { u64 d_; asm volatile("s_load_dwordx2 %0, %1, %2\n\ts_load_dwordx2 %0, %1, %2+256\n\ts_load_dwordx2 %0, %1, %2+512\n\ts_load_dwordx2 %0, %1, %2+768\n\t" \
        "s_load_dwordx2 %0, %1, %2+1024\n\ts_load_dwordx2 %0, %1, %2+1280\n\ts_load_dwordx2 %0, %1, %2+1536\n\ts_load_dwordx2 %0, %1, %2+1792" \
        : "=&s"(sd) : "s"(A + (size_t)(bid * 8 + wid) * K_), "i"(60 + 128 * (par)) : "memory"); }
#define SKEEP asm volatile("s_waitcnt lgkmcnt(0)" :: "s"(sd));
#define XTODD asm volatile("s_mov_b64 exec, 0xffff\n\tglobal_load_dword %0, %1, %2\n\ts_mov_b64 exec, -1" : "+v"(tv) : "v"((unsigned)(bid * 128 + hw * 32 + (lane & 15) * 2 + 1) * 128u), "s"(A) : "memory");
#define XTEVEN8 asm volatile("s_mov_b64 exec, 0xff\n\tglobal_load_dword %0, %1, %2\n\ts_mov_b64 exec, -1" : "+v"(tv) : "v"((unsigned)(bid * 128 + wid * 16 + (lane & 7) * 2) * 128u), "s"(A) : "memory");
#define WSHIFT _Pragma("unroll") for (int c = 0; c < 4; ++c) woff[c] += dp * PL + (CR ? (pm & 1) * 8 : 0);
#define WFLIP _Pragma("unroll") for (int c = 0; c < 4; ++c) woff[c] ^= 8;
#define XT2(par) asm volatile("s_mov_b32 exec_hi, 0\n\tglobal_load_dword %0, %1, %2\n\ts_mov_b32 exec_hi, -1" : "+v"(tv) : "v"((unsigned)(((bid >> 4) * 8 + (bid & 7)) * 256 + hw * 64 + (lane & 31) * 2 + (par)) * 128u), "s"(A) : "memory");
#define XT2A asm volatile("global_load_dword %0, %1, %2" : "+v"(tv) : "v"((unsigned)(((bid >> 4) * 8 + (bid & 7)) * 256 + hw * 64 + lane) * 128u), "s"(A) : "memory");
#define QUNDEF asm volatile("" : "=v"(q3[0]), "=v"(q3[1]), "=v"(q3[2]), "=v"(q3[3]), "=v"(q4[0]), "=v"(q4[1]), "=v"(q4[2]), "=v"(q4[3]));
#define PUT2(off) { const u32x2 tg = {tlo, thi}; const unsigned fo = (off) + (lane & 1) * 128; \
        asm volatile("s_mov_b64 exec, 3\n\tglobal_atomic_swap_x2 %0, %1, %2 sc1\n\ts_mov_b64 exec, -1" :: "v"(fo), "v"(tg), "s"(hand) : "memory"); }
    XSTART
    __builtin_amdgcn_s_barrier();
    QLOAD(q2, qw + 4 * N_)
    PGC
    SETUP

    if (helper) {
        char* Wg = Wp + hw * (2 * PL) + 3072;
#define WTILEK(k_, bf) \
        asm volatile("s_mov_b32 m0, %0\n\ts_nop 0\n\t" \
                     "global_load_lds_dwordx4 %2, %3 sc1\n\tglobal_load_lds_dwordx4 %2, %3 offset:1024 sc1\n\t" \
                     "global_load_lds_dwordx4 %2, %3 offset:2048 sc1\n\tglobal_load_lds_dwordx4 %2, %3 offset:3072 sc1\n\t" \
                     "s_mov_b32 m0, %1\n\ts_nop 0\n\t" \
                     "global_load_lds_dwordx4 %2, %4 sc1\n\tglobal_load_lds_dwordx4 %2, %4 offset:1024 sc1\n\tglobal_load_lds_dwordx4 %2, %4 offset:2048 sc1" \
                     :: "s"(lds0 + W_OFF + (bf) * WB + hw * (2 * PL) + 3072), "s"(lds0 + W_OFF + (bf) * WB + hw * (2 * PL)), "v"(l16), \
                        "s"(Wg + (k_) * WB), "s"(Wg + (k_) * WB - 3072) : "memory");
#define SNAP(sn, tt) asm volatile("s_lshr_b32 %0, %1, %2" : "=s"(sn) : "s"(rdy), "s"(KT(tt)) : "scc");
#define WAITC(n, sn) asm volatile("s_waitcnt vmcnt(%0)\n\ts_bitcmp1_b32 %2, 0\n\ts_cbranch_scc1 9f\n\ts_waitcnt vmcnt(%1)\n9:" :: "i"((n) + 7), "i"(n), "s"(sn) : "memory", "scc");
#define WTILEIF(tt, sn, c) { int k_ = KT(tt); asm volatile("" : "+s"(k_)); \
        asm volatile("s_bitcmp%6_b32 %5, 0\n\ts_cbranch_scc0 9f\n\ts_mov_b32 m0, %0\n\ts_nop 0\n\t" \
                     "global_load_lds_dwordx4 %2, %3 sc1\n\tglobal_load_lds_dwordx4 %2, %3 offset:1024 sc1\n\t" \
                     "global_load_lds_dwordx4 %2, %3 offset:2048 sc1\n\tglobal_load_lds_dwordx4 %2, %3 offset:3072 sc1\n\t" \
                     "s_mov_b32 m0, %1\n\ts_nop 0\n\t" \
                     "global_load_lds_dwordx4 %2, %4 sc1\n\tglobal_load_lds_dwordx4 %2, %4 offset:1024 sc1\n\tglobal_load_lds_dwordx4 %2, %4 offset:2048 sc1\n9:" \
                     :: "s"(lds0 + W_OFF + (tt) % 3 * WB + hw * (2 * PL) + 3072), "s"(lds0 + W_OFF + (tt) % 3 * WB + hw * (2 * PL)), "v"(l16), \
                        "s"(Wg + (k_) * WB), "s"(Wg + (k_) * WB - 3072), "s"(sn), "i"(c) : "memory", "scc"); }
#define WTILE(tt) { int k_ = KT(tt); asm volatile("" : "+s"(k_)); WTILEK(k_, (tt) % 3) }
#define HLOOP(u0, u1) { u64 ktab = ktab0 >> (4 * ((u0) + 1)); unsigned bf = ((u0) + 2) % 3; \
            _Pragma("nounroll") for (int u = (u0); u < (u1); ++u) { \
                const unsigned k_ = (unsigned)ktab & 15u; \
                WTILEK(k_, bf) XTILEK(k_, bf) if (XJD) XJK((unsigned)(ktab >> (4 * XJD)) & 15u) \
                ktab >>= 4; bf = (0x9u >> (2 * bf)) & 3u; \
                wait_vm<11>(); __builtin_amdgcn_s_barrier(); } }
        unsigned gw;
#define OTOUCH asm volatile("s_load_dwordx2 %0, %1, 0" : "+&s"(sd) : "s"(C + (size_t)(brow + 42 * hw) * N_ + bcol) : "memory");
#define WPC(t_, i, a_) asm volatile("s_mov_b32 m0, %0\n\ts_nop 0\n\tglobal_load_lds_dwordx4 %1, %2 sc1" :: "s"(lds0 + hw * 1024 + (a_)), "v"(gw), "s"(Wg - 3072 + KT(t_) * WB + (i) * 256) : "memory");
#define XPART(t_, h_, a_, q_) asm volatile("s_mov_b32 m0, %0\n\ts_nop 0\n\tglobal_load_lds_dwordx4 %1, %3\n\tglobal_load_lds_dwordx4 %2, %3 offset:1024" \
            :: "s"(lds0 + (a_) + ((hw & 2) + (h_)) * (q_) + (hw & 1) * 2048), "v"(dv[0]), "v"(dv[1]), "s"(A + (size_t)(brow + 64 * (hw >> 1) + 16 * (hw & 1) + 32 * (h_)) * K_ + KT(t_) * BK) : "memory");
#define XPQ(t_, h_, a_, q_, i_) asm volatile("s_mov_b32 m0, %0\n\ts_nop 0\n\tglobal_load_lds_dwordx4 %1, %2 offset:%3" \
            :: "s"(lds0 + (a_) + ((hw & 2) + (h_)) * (q_) + (hw & 1) * 2048), "v"(dv[i_]), "s"(A + (size_t)(brow + 64 * (hw >> 1) + 16 * (hw & 1) + 32 * (h_)) * K_ + KT(t_) * BK), "i"((i_) * 1024) : "memory");
        const u16* Ah = A + (size_t)(brow + 64 * (hw >> 1) + 16 * (hw & 1)) * K_ + KT(15) * BK;
#define XHD asm volatile("s_mov_b32 m0, %0\n\ts_nop 0\n\tglobal_load_lds_dwordx4 %1, %3\n\tglobal_load_lds_dwordx4 %2, %3 offset:1024" \
                     :: "s"(lds0 + (hw >> 1) * 24576 + (hw & 1) * 2048 + 12288), "v"(dv[0]), "v"(dv[1]), "s"(Ah + 32 * K_) : "memory");
#define XJL(ro) asm volatile("s_load_dwordx2 %0, %1, 60\n\ts_load_dwordx2 %0, %1, 2108\n\ts_load_dwordx2 %0, %1, 4156\n\ts_load_dwordx2 %0, %1, 6204" : "+&s"(sd) : "s"(Ah + ((ro) + (pn & 3) * 4) * K_) : "memory");
#define XJT(t_, ro) asm volatile("s_load_dwordx2 %0, %1, %2 offset:60\n\ts_load_dwordx2 %0, %1, %2 offset:2108\n\ts_load_dwordx2 %0, %1, %2 offset:4156\n\ts_load_dwordx2 %0, %1, %2 offset:6204" : "+&s"(sd) \
            : "s"(A + (size_t)(brow + 64 * (hw >> 1) + 16 * (hw & 1) + (pn & 3) * 4) * K_), "s"(KT(t_) * 128 + (ro) * 2048) : "memory");
#define XLD asm volatile("s_mov_b32 m0, %0\n\ts_nop 0\n\tglobal_load_lds_dwordx4 %1, %3\n\tglobal_load_lds_dwordx4 %2, %3 offset:1024" \
                     :: "s"(lds0 + (hw >> 1) * 24576 + (hw & 1) * 2048), "v"(dv[0]), "v"(dv[1]), "s"(Ah) : "memory");
        unsigned rdy = 0;
        const unsigned po = 256 + hw * 256 + (lane & 31) * 8;
        u32x2 pf = {0u, 0u};
        const u32x2 pz = {0u, 0u};
#define APOLL asm volatile("s_mov_b32 exec_hi, 0\n\tglobal_atomic_or_x2 %0, %1, %2, %3 sc0 sc1\n\ts_mov_b32 exec_hi, -1" : "+v"(pf) : "v"(po), "v"(pz), "s"(hand) : "memory");
#define ALOOK(n) { asm volatile("s_waitcnt vmcnt(%1)" : "+v"(pf) : "i"(n) : "memory"); \
                const unsigned b_ = (unsigned)__builtin_amdgcn_ballot_w64(pf[0] == tlo && pf[1] == thi); rdy |= b_ & (b_ >> 16); }
#define APOLLM(m) asm volatile("s_mov_b64 exec, %4\n\tglobal_atomic_or_x2 %0, %1, %2, %3 sc0 sc1\n\ts_mov_b64 exec, -1" : "+v"(pf) : "v"(po), "v"(pz), "s"(hand), "s"((u64)((m) * 0x10001u)) : "memory");
#define ALOOKM(n, m) { asm volatile("s_waitcnt vmcnt(%1)" : "+v"(pf) : "i"(n) : "memory"); \
                const unsigned b_ = (unsigned)__builtin_amdgcn_ballot_w64(pf[0] == tlo && pf[1] == thi); rdy |= b_ & (b_ >> 16) & (m); }
        u32x2 pg = {0u, 0u};
#define APOLL2 asm volatile("s_mov_b32 exec_hi, 0\n\tglobal_atomic_or_x2 %0, %1, %2, %3 sc0 sc1\n\ts_mov_b32 exec_hi, -1" : "+v"(pg) : "v"(po), "v"(pz), "s"(hand) : "memory");
#define APOLL2M(m) asm volatile("s_mov_b64 exec, %4\n\tglobal_atomic_or_x2 %0, %1, %2, %3 sc0 sc1\n\ts_mov_b64 exec, -1" : "+v"(pg) : "v"(po), "v"(pz), "s"(hand), "s"((u64)((m) * 0x10001u)) : "memory");
#define ALOOK2(m, n, pm_) { unsigned t_; asm volatile("s_and_b32 %2, %1, %4\n\ts_cbranch_scc1 2f\n\ts_waitcnt vmcnt(%5)\n\tv_cmp_eq_u64 vcc, %3, %0\n\ts_nop 1\n\ts_lshr_b32 %2, vcc_lo, 16\n\ts_and_b32 %2, %2, vcc_lo\n\ts_and_b32 %2, %2, %6\n\ts_or_b32 %1, %1, %2\n2:" \
                         : "+v"(pg), "+&s"(rdy), "=&s"(t_) : "s"(tag), "s"(m), "i"(n), "s"(pm_) : "memory", "scc", "vcc"); }
#define NEEDX(m, pm_) { unsigned t_, n_; u32x2 f_; \
            asm volatile("s_mov_b32 %3, 0x40000\n\ts_mov_b64 exec, %9\n1:\n\ts_andn2_b32 %2, %8, %1\n\ts_cbranch_scc0 2f\n\ts_sub_u32 %3, %3, 1\n\ts_cbranch_scc1 2f\n\t" \
                         "global_atomic_or_x2 %0, %4, %5, %6 sc0 sc1\n\ts_waitcnt vmcnt(0)\n\tv_cmp_eq_u64 vcc, %7, %0\n\ts_nop 1\n\t" \
                         "s_lshr_b32 %2, vcc_lo, 16\n\ts_and_b32 %2, %2, vcc_lo\n\ts_or_b32 %1, %1, %2\n\ts_branch 1b\n2:\n\ts_mov_b64 exec, -1" \
                         : "=&v"(f_), "+&s"(rdy), "=&s"(t_), "=&s"(n_) \
                         : "v"(po), "v"(pz), "s"(hand), "s"(tag), "s"(m), "s"((u64)((pm_) * 0x10001u)) : "memory", "scc", "vcc"); }
#define NEED(m) NEEDX(m, m)
        u32x2 pa = pz, pc = pz;
#define PCHK(R) "s_waitcnt vmcnt(1)\n\tv_cmp_eq_u64 vcc, %8, " R "\n\ts_nop 1\n\ts_lshr_b32 %3, vcc_lo, 16\n\ts_and_b32 %3, %3, vcc_lo\n\ts_or_b32 %2, %2, %3\n\ts_and_b32 %3, %2, %9\n\ts_cbranch_scc1 2f\n\t"
#define NEEDP(m, pm_) { unsigned t_, n_; \
            asm volatile("s_and_b32 %3, %2, %9\n\ts_cbranch_scc1 2f\n\ts_mov_b32 %4, 0x20000\n\ts_mov_b64 exec, %10\n\tglobal_atomic_or_x2 %0, %5, %6, %7 sc0 sc1\n1:\n\ts_sleep %11\n\t" \
                         "global_atomic_or_x2 %1, %5, %6, %7 sc0 sc1\n\t" PCHK("%0") \
                         "global_atomic_or_x2 %0, %5, %6, %7 sc0 sc1\n\t" PCHK("%1") \
                         "s_sub_u32 %4, %4, 1\n\ts_cbranch_scc0 1b\n2:\n\ts_mov_b64 exec, -1" \
                         : "+&v"(pa), "+&v"(pc), "+&s"(rdy), "=&s"(t_), "=&s"(n_) \
                         : "v"(po), "v"(pz), "s"(hand), "s"(tag), "s"(m), "s"((u64)((pm_) * 0x10001u)), "i"(PSL2) : "memory", "scc", "vcc"); }
#define ADVANCE { const u32x2 tg = {tlo, thi}; \
            asm volatile("s_cmp_eq_u32 %3, 0\n\ts_cselect_b64 exec, 1, 0\n\tglobal_atomic_swap_x2 %0, %1, %2 sc1\n\ts_mov_b64 exec, -1" :: "v"(zero), "v"(tg), "s"(hand), "s"(pm | hw) : "memory", "scc"); }
#define ADVB { const u32x2 tg = {tlo, thi}; \
            asm volatile("s_cmp_eq_u32 %3, 0\n\ts_cbranch_scc0 1f\n\ts_mov_b64 exec, 1\n\tglobal_atomic_swap_x2 %0, %1, %2 sc1\n\ts_mov_b64 exec, -1\n1:" :: "v"(zero), "v"(tg), "s"(hand), "s"(pm | hw) : "memory", "scc"); }
#define DBG(k)
#define PKEEP asm volatile("" :: "v"(pa), "v"(pc), "v"(pg));
        bool adv = pm != 0 || hw != 0;
#define EPOCH_TRY if (!adv && rdy == 0xFFFFu) { PUTWORD(0) adv = true; }
        unsigned lh, ch;
        u32x4 sA[4], sB[4], sC[4];
#define COADDR { SB(); unsigned ln = lane; asm volatile("" : "+v"(ln)); const unsigned r0 = (hw >> 1) * 64 + (hw & 1) * 16; \
            lh = lds0 + ST_OFF + 16384 + r0 * 64 + ln * 16; ch = (odd ? 0 : 384) + ((ln >> 2) + 16 * (hw & 1)) * (2 * N_) + ((ln ^ (ln >> 3)) & 3) * 16; \
            gw = (ln >> 4) * 1536 + ln * 16; asm volatile("" : "+v"(lh), "+v"(ch), "+v"(gw)); SB(); }
#define UREAD(sv, l_, lo_, c_, co_) DSREAD(sv, l_, lo_);
#define USTORE(sv, l_, lo_, c_, co_, w, ro, OP) asm volatile("s_waitcnt lgkmcnt(%4)\n\tglobal_store_dwordx4 %0, %1, %2 offset:%3 " OP "\n\ts_nop 1" \
            :: "v"(c_), "v"(sv), "s"(C + (size_t)(brow + 64 * (hw >> 1) + (ro)) * N_ + bcol), "i"(co_), "i"(w) : "memory");
#define HEND(l) asm volatile("" :: "v"(tv)); asm volatile(l "\n\ts_endpgm\n" PG1); __builtin_unreachable();
        O_E
        wait_vm<9>();
        LUTW4
        LADD
        TS(1)
        LWAIT(4)
        TS(19)
        XTILE(0)
        TS(20)
        wait_vm<9>();
        TS(2)
        DEQUANT(q, 0)
        TS(21)
        QLOADX(q3, BK)
        TS(22)
        wait_vm<9>();
        TS(11)
        DEQUANTH(q2, 8, HNONE, 0)
        wait_vm<4>();
        EPPUT
        LADD
        TS(3)
        PUBP("", ap * PL, so, ap)
        TS(12)
        QLOADX(q4, BK + 4)
        TS(4)
        WSHIFT
        wait_vm<8>();
        TS(24)
        DEQUANT(q3, BB * WB)
        TS(25)
        LWAIT(12)
        GETTAG
        wait_vm<4>();
        PUTWORD(256 + ap * 128 + so * 8)
        TS(15)
        XTILE(1)
        TS(7)
        wait_vm<5>();
        WFLIP DEQUANT(q4, BB * WB)
        TS(8)
        APOLLM((1u << KT(2)) | (1u << KT(3)) | (1u << KT(4)))
        XTILE(2)
        TS(13)
        COADDR
        wait_vm<5>();
        CADD
        TS(5)
        ALOOKM(4, (1u << KT(2)) | (1u << KT(3)) | (1u << KT(4)))
        TS(26)
        NEEDP(1u << KT(2), (1u << KT(2)) | (1u << KT(3)) | (1u << KT(4)))
        TS(14)
        PGH
        WTILE(2)
        APOLLM(0x5555u)
        CWAIT
        TS(6)
        NEED(1u << KT(3))
        WTILE(3)
        XTILE(3)
        PUBR(BB * WB + (2 * hw + (pm & 1 ^ 1)) * PL)
        PUBW(so + 1, 2 * hw + (pm & 1 ^ 1))
        TS(17)
        wait_vm<16>();
        PKEEP
        TS(33)
        wait_lgkm<0>(); __builtin_amdgcn_s_barrier();
        XTILE(4)
        ALOOKM(19, 0x5555u)
        NEED(1u << KT(4))
        WTILE(4)
        TS(18)
        XJK(KT(6))
        XJK(KT(7))
        wait_vm<15>();
        __builtin_amdgcn_s_barrier();
        NEED(0x5555u)
        WTILE(5)
        XTILE(5)
        wait_vm<22>();
        PUTWORD(256 + (2 * hw + (pm & 1 ^ 1)) * 128 + so * 8 + 8)
        XJK(KT(8))
        wait_vm<12>();
        __builtin_amdgcn_s_barrier();
        WTILE(6)
        XTILE(6)
        XJK(KT(9))
        OTOUCH
        wait_vm<12>();
        __builtin_amdgcn_s_barrier();
        WTILE(7)
        XTILE(7)
        XJK(KT(10))
        XJK(KT(15))
        wait_vm<11>();
        __builtin_amdgcn_s_barrier();
        APOLLM(0xAAAAu)
        WTILE(8)
        XTILE(8)
        XJK(KT(11))
        wait_vm<12>();
        __builtin_amdgcn_s_barrier();
        ALOOKM(11, 0xAAAAu)
        NEED(0xFFFFu)
        ADVB
        WTILE(9)
        XTILE(9)
        XJK(KT(12))
        wait_vm<11>();
        __builtin_amdgcn_s_barrier();
        WTILE(10)
        XTILE(10)
        WPC(15, 0, 151552)
        WPC(15, 1, 155648)
        XJK(KT(13))
        wait_vm<13>();
        __builtin_amdgcn_s_barrier();
        WTILE(11)
        XTILE(11)
        XPART(15, 0, 135168, 4096)
        WPC(14, 1, 159744)
        XJK(KT(14))
        wait_vm<16>();
        __builtin_amdgcn_s_barrier();
        WTILE(12)
        XTILE(12)
        XPART(15, 1, 135168, 4096)
        wait_vm<16>();
        __builtin_amdgcn_s_barrier();
        XPART(13, 0, 4096, 12288)
        WPC(13, 0, 77824)
        WPC(13, 1, 81920)
        XPART(14, 0, 90112, 4096)
        WPC(14, 0, 86016)
        XPART(13, 1, 4096, 12288)
        XPART(14, 1, 90112, 4096)
        wait_vm<13>();
        __builtin_amdgcn_s_barrier();
        WPC(13, 2, 106496)
        WPC(14, 2, 110592)
        WPC(15, 2, 114688)
        PGH2
        WPC(13, 3, 118784)
        WPC(14, 3, 122880)
        WPC(15, 3, 126976)
        WPC(13, 6, 131072)
        WPC(14, 6, 8192)
        WPC(15, 6, 20480)
        wait_vm<9>();
        __builtin_amdgcn_s_barrier();
        WPC(13, 4, 32768)
        WPC(14, 4, 45056)
        WPC(15, 4, 0)
        WPC(13, 5, 12288)
        WPC(14, 5, 24576)
        WPC(15, 5, 36864)
        wait_vm<9>();
        __builtin_amdgcn_s_barrier();
        wait_vm<0>();
        SKEEP
        __builtin_amdgcn_s_barrier();
        __builtin_amdgcn_s_barrier();
        UREAD(sA[0], lh, 0, ch, 0)
        USTORE(sA[0], lh, 0, ch, 0, 0, 0, "sc1")
        __builtin_amdgcn_s_barrier();
        UREAD(sB[0], lh, 2048, ch, 0)
        USTORE(sB[0], lh, 2048, ch, 0, 0, 32, "sc1")
        HEND("sq_k_end1:")
    } else {
        const int r = lane & 15, g = lane >> 4, wr = hw >> 1, wc = hw & 1;
        unsigned xa[2], wa[2];
        const unsigned sg = (r >> 1) * 16 + (r & 1) * 128;
#pragma unroll
        for (int i = 0; i < 2; ++i) {
            xa[i] = lds0 + wr * 24576 + r * 128 + (((4 * i + g) ^ ((r >> 1) & 7)) << 4);
            wa[i] = lds0 + W_OFF + (4 * i + g) * PL + wc * (PL / 2) + sg;
        }
        const unsigned wl = lds0 + W_OFF + 2 * WB + g * 512 + wc * 256 + sg;
        const unsigned st = lds0 + ST_OFF + 16384 + (wr * 64 + r) * 64 + (((2 * wc + (g >> 1)) ^ ((r >> 1) & 3)) << 4) + (g & 1) * 8;
#define RECIN
#define RECFL
#define Q4IN "{v232}"(qv[0]), "{v236}"(qv[1]), "{v240}"(qv[2]), "{v244}"(qv[3]), "{s[56:57]}"(qw + (ptrdiff_t)(CRO(BK + 4) + dp * 8) * N_),
#define BODYIN "{v112}"(xa[0]), "{v113}"(xa[1]), "{v114}"(wa[0]), "{v115}"(wa[1]), "{v116}"(wl), \
               "{v121}"(st), "{v122}"(tv), "{s52}"(W_OFF + WB + 12288 - wr * 16384), "{v124}"(g * (8 * N_) + r * 4), "{s[64:65]}"(C + (size_t)(brow + 64 * wr) * N_ + bcol + (wc + odd) * 32), \
               "{v[216:219]}"(q3[0]), "{v[220:223]}"(q3[1]), "{v[224:227]}"(q3[2]), "{v[228:231]}"(q3[3]), Q4IN \
               "{v248}"(woff[0]), "{v249}"(woff[1]), "{v250}"(woff[2]), "{v251}"(woff[3]), "{v252}"(lbase), "{v253}"(cadr) RECIN
#define CXIN , "{v255}"(dv[0]), "{s[56:57]}"(Aw), "{s58}"(lds0 + hw * 12288), "{s59}"((unsigned)ktab0 << 4)
        TS(1)
        wait_vm<4>();
        LWAIT(4)
        TS(2)
        DEQUANT(q, 0)
        wait_vm<0>();
        TS(11)
        DEQUANTH(q2, 8, HNONE, 0)
        LADD
        WSHIFT
        TS(12)
        QLOAD3
        LWAIT(12)
        TS(4)
        TS(3)
        RECFL
            asm volatile(
            "ds_read_b128 v[128:131], v114 offset:0\n"
            "ds_read_b128 v[156:159], v112 offset:0\n"
            "ds_read_b128 v[172:175], v115 offset:0\n"
            "ds_read_b128 v[200:203], v113 offset:0\n"
            "ds_read_b128 v[176:179], v115 offset:256\n"
            "ds_read_b128 v[132:135], v114 offset:256\n"
            "ds_read_b128 v[160:163], v112 offset:2048\n"
            "ds_read_b128 v[204:207], v113 offset:2048\n"
            "ds_read_b128 v[164:167], v112 offset:12288\n"
            "ds_read_b128 v[208:211], v113 offset:12288\n"
            "ds_read_b128 v[168:171], v112 offset:14336\n"
            "ds_read_b128 v[212:215], v113 offset:14336\n"
            "s_waitcnt lgkmcnt(10)\n"
            "v_mfma_f32_16x16x32_bf16 v[0:3], v[156:159], v[128:131], 0\n"
            "s_waitcnt lgkmcnt(8)\n"
            "v_mfma_f32_16x16x32_bf16 v[0:3], v[200:203], v[172:175], v[0:3]\n"
            "s_waitcnt lgkmcnt(7)\n"
            "v_mfma_f32_16x16x32_bf16 v[4:7], v[200:203], v[176:179], 0\n"
            "s_waitcnt lgkmcnt(6)\n"
            "v_mfma_f32_16x16x32_bf16 v[4:7], v[156:159], v[132:135], v[4:7]\n"
            "ds_read_b128 v[136:139], v114 offset:512\n"
            "s_waitcnt lgkmcnt(6)\n"
            "v_mfma_f32_16x16x32_bf16 v[32:35], v[160:163], v[132:135], 0\n"
            "s_waitcnt lgkmcnt(5)\n"
            "v_mfma_f32_16x16x32_bf16 v[32:35], v[204:207], v[176:179], v[32:35]\n"
            "ds_read_b128 v[180:183], v115 offset:512\n"
            "v_mfma_f32_16x16x32_bf16 v[28:31], v[204:207], v[172:175], 0\n"
            "v_mfma_f32_16x16x32_bf16 v[28:31], v[160:163], v[128:131], v[28:31]\n"
            "ds_read_b128 v[140:143], v114 offset:768\n"
            "s_waitcnt lgkmcnt(3)\n"
            "v_mfma_f32_16x16x32_bf16 v[56:59], v[164:167], v[128:131], 0\n"
            "v_mfma_f32_16x16x32_bf16 v[56:59], v[208:211], v[172:175], v[56:59]\n"
            "ds_read_b128 v[184:187], v115 offset:768\n"
            "v_mfma_f32_16x16x32_bf16 v[60:63], v[208:211], v[176:179], 0\n"
            "v_mfma_f32_16x16x32_bf16 v[60:63], v[164:167], v[132:135], v[60:63]\n"
            "ds_read_b128 v[144:147], v114 offset:1024\n"
            "v_mfma_f32_16x16x32_bf16 v[88:91], v[168:171], v[132:135], 0\n"
            "v_mfma_f32_16x16x32_bf16 v[88:91], v[212:215], v[176:179], v[88:91]\n"
            "ds_read_b128 v[188:191], v115 offset:1024\n"
            "v_mfma_f32_16x16x32_bf16 v[84:87], v[212:215], v[172:175], 0\n"
            "v_mfma_f32_16x16x32_bf16 v[84:87], v[168:171], v[128:131], v[84:87]\n"
            "ds_read_b128 v[148:151], v114 offset:1280\n"
            "s_waitcnt lgkmcnt(3)\n"
            "v_mfma_f32_16x16x32_bf16 v[92:95], v[168:171], v[136:139], 0\n"
            "v_mfma_f32_16x16x32_bf16 v[92:95], v[212:215], v[180:183], v[92:95]\n"
            "ds_read_b128 v[192:195], v115 offset:1280\n"
            "v_mfma_f32_16x16x32_bf16 v[64:67], v[208:211], v[180:183], 0\n"
            "v_mfma_f32_16x16x32_bf16 v[64:67], v[164:167], v[136:139], v[64:67]\n"
            "ds_read_b128 v[196:199], v115 offset:1536\n"
            "v_mfma_f32_16x16x32_bf16 v[36:39], v[160:163], v[136:139], 0\n"
            "v_mfma_f32_16x16x32_bf16 v[36:39], v[204:207], v[180:183], v[36:39]\n"
            "ds_read_b128 v[152:155], v114 offset:1536\n"
            "v_mfma_f32_16x16x32_bf16 v[8:11], v[200:203], v[180:183], 0\n"
            "v_mfma_f32_16x16x32_bf16 v[8:11], v[156:159], v[136:139], v[8:11]\n"
            "v_mfma_f32_16x16x32_bf16 v[12:15], v[156:159], v[140:143], 0\n"
            "v_mfma_f32_16x16x32_bf16 v[12:15], v[200:203], v[184:187], v[12:15]\n"
            "v_mfma_f32_16x16x32_bf16 v[40:43], v[204:207], v[184:187], 0\n"
            "global_load_dwordx4 v[232:235], v232, s[56:57] nt\n"
            "v_mfma_f32_16x16x32_bf16 v[40:43], v[160:163], v[140:143], v[40:43]\n"
            "v_mfma_f32_16x16x32_bf16 v[68:71], v[164:167], v[140:143], 0\n"
            "v_mfma_f32_16x16x32_bf16 v[68:71], v[208:211], v[184:187], v[68:71]\n"
            "global_load_dwordx4 v[236:239], v236, s[56:57] nt\n"
            "v_mfma_f32_16x16x32_bf16 v[96:99], v[212:215], v[184:187], 0\n"
            "v_mfma_f32_16x16x32_bf16 v[96:99], v[168:171], v[140:143], v[96:99]\n"
            "s_waitcnt lgkmcnt(0)\n"
            "v_mfma_f32_16x16x32_bf16 v[100:103], v[168:171], v[144:147], 0\n"
            "global_load_dwordx4 v[240:243], v240, s[56:57] nt\n"
            "v_mfma_f32_16x16x32_bf16 v[100:103], v[212:215], v[188:191], v[100:103]\n"
            "v_mfma_f32_16x16x32_bf16 v[72:75], v[208:211], v[188:191], 0\n"
            "v_mfma_f32_16x16x32_bf16 v[72:75], v[164:167], v[144:147], v[72:75]\n"
            "global_load_dwordx4 v[244:247], v244, s[56:57] nt\n"
            "v_mfma_f32_16x16x32_bf16 v[44:47], v[160:163], v[144:147], 0\n"
            "v_mfma_f32_16x16x32_bf16 v[44:47], v[204:207], v[188:191], v[44:47]\n"
            "v_mfma_f32_16x16x32_bf16 v[16:19], v[200:203], v[188:191], 0\n"
            "v_mfma_f32_16x16x32_bf16 v[16:19], v[156:159], v[144:147], v[16:19]\n"
            "v_mfma_f32_16x16x32_bf16 v[20:23], v[156:159], v[148:151], 0\n"
            "v_mfma_f32_16x16x32_bf16 v[20:23], v[200:203], v[192:195], v[20:23]\n"
            "v_mfma_f32_16x16x32_bf16 v[24:27], v[196:199], v[200:203], 0\n"
            "v_mfma_f32_16x16x32_bf16 v[24:27], v[152:155], v[156:159], v[24:27]\n"
            "v_mfma_f32_16x16x32_bf16 v[52:55], v[152:155], v[160:163], 0\n"
            "v_mfma_f32_16x16x32_bf16 v[52:55], v[196:199], v[204:207], v[52:55]\n"
            "v_mfma_f32_16x16x32_bf16 v[48:51], v[204:207], v[192:195], 0\n"
            "v_mfma_f32_16x16x32_bf16 v[48:51], v[160:163], v[148:151], v[48:51]\n"
            "v_mfma_f32_16x16x32_bf16 v[76:79], v[164:167], v[148:151], 0\n"
            "v_mfma_f32_16x16x32_bf16 v[76:79], v[208:211], v[192:195], v[76:79]\n"
            "v_mfma_f32_16x16x32_bf16 v[80:83], v[196:199], v[208:211], 0\n"
            "v_mfma_f32_16x16x32_bf16 v[80:83], v[152:155], v[164:167], v[80:83]\n"
            "v_mfma_f32_16x16x32_bf16 v[108:111], v[152:155], v[168:171], 0\n"
            "v_mfma_f32_16x16x32_bf16 v[108:111], v[196:199], v[212:215], v[108:111]\n"
            "v_mfma_f32_16x16x32_bf16 v[104:107], v[212:215], v[192:195], 0\n"
            "v_mfma_f32_16x16x32_bf16 v[104:107], v[168:171], v[148:151], v[104:107]\n"
            "s_branch 7f\n"
            ".p2align 12\n"
            "sq_pg2:\n"
            "s_setpc_b64 s[60:61]\n"
            "7:\n"
            "v_mov_b32 v254, 16\n"
            "s_mov_b64 exec, 1\n"
            "ds_add_u32 v253, v254\n"
            "s_mov_b64 exec, -1\n"
            "v_add_u32 v117, -106496, v116\n"
            "v_add_u32 v120, -28672, v116\n"
            "v_add_u32 v118, s52, v112\n"
            "v_add_u32 v119, s52, v113\n"
            "v_add_u32 v125, 7168, v124\n"
            "v_add_u32 v126, 14336, v124\n"
            "v_add_u32 v127, 21504, v124\n"
            "s_add_u32 s66, s64, 114688\n"
            "s_addc_u32 s67, s65, 0\n"
            "s_add_u32 s68, s64, 229376\n"
            "s_addc_u32 s69, s65, 0\n"
            "s_add_u32 s70, s64, 344064\n"
            "s_addc_u32 s71, s65, 0\n"
            "s_mov_b32 s42, 0x05040100\n"
            "s_mov_b32 s43, 0x07060302\n"
            "s_waitcnt vmcnt(4)\n"
            "v_lshl_add_u32 v216, v216, 9, v252\n"
            "v_lshl_add_u32 v220, v220, 9, v252\n"
            "v_lshl_add_u32 v224, v224, 9, v252\n"
            "v_lshl_add_u32 v228, v228, 9, v252\n"
            "ds_read_b32 v216, v216 offset:0\n"
            "ds_read_b32 v220, v220 offset:0\n"
            "ds_read_b32 v224, v224 offset:0\n"
            "ds_read_b32 v228, v228 offset:0\n"
            "v_lshl_add_u32 v217, v217, 9, v252\n"
            "v_lshl_add_u32 v221, v221, 9, v252\n"
            "v_lshl_add_u32 v225, v225, 9, v252\n"
            "v_lshl_add_u32 v229, v229, 9, v252\n"
            "ds_read_b32 v217, v217 offset:0\n"
            "ds_read_b32 v221, v221 offset:0\n"
            "ds_read_b32 v225, v225 offset:0\n"
            "ds_read_b32 v229, v229 offset:0\n"
            "v_lshl_add_u32 v218, v218, 9, v252\n"
            "v_lshl_add_u32 v222, v222, 9, v252\n"
            "v_lshl_add_u32 v226, v226, 9, v252\n"
            "v_lshl_add_u32 v230, v230, 9, v252\n"
            "ds_read_b32 v218, v218 offset:256\n"
            "ds_read_b32 v222, v222 offset:256\n"
            "ds_read_b32 v226, v226 offset:256\n"
            "ds_read_b32 v230, v230 offset:256\n"
            "v_lshl_add_u32 v219, v219, 9, v252\n"
            "v_lshl_add_u32 v223, v223, 9, v252\n"
            "v_lshl_add_u32 v227, v227, 9, v252\n"
            "v_lshl_add_u32 v231, v231, 9, v252\n"
            "ds_read_b32 v219, v219 offset:256\n"
            "ds_read_b32 v223, v223 offset:256\n"
            "ds_read_b32 v227, v227 offset:256\n"
            "ds_read_b32 v231, v231 offset:256\n"
            "s_waitcnt lgkmcnt(12)\n"
            "v_perm_b32 v128, v220, v216, s42\n"
            "v_perm_b32 v129, v228, v224, s42\n"
            "ds_write_b64 v248, v[128:129] offset:28672\n"
            "s_waitcnt lgkmcnt(9)\n"
            "v_perm_b32 v130, v221, v217, s43\n"
            "v_perm_b32 v131, v229, v225, s43\n"
            "ds_write_b64 v249, v[130:131] offset:28672\n"
            "s_waitcnt lgkmcnt(6)\n"
            "v_perm_b32 v132, v222, v218, s42\n"
            "v_perm_b32 v133, v230, v226, s42\n"
            "ds_write_b64 v250, v[132:133] offset:28672\n"
            "s_waitcnt lgkmcnt(3)\n"
            "v_perm_b32 v134, v223, v219, s43\n"
            "v_perm_b32 v135, v231, v227, s43\n"
            "ds_write_b64 v251, v[134:135] offset:28672\n"
            "s_waitcnt lgkmcnt(0)\n"
            "s_waitcnt vmcnt(0)\n"
            "v_xor_b32 v248, 8, v248\n"
            "v_xor_b32 v249, 8, v249\n"
            "v_xor_b32 v250, 8, v250\n"
            "v_xor_b32 v251, 8, v251\n"
            "v_lshl_add_u32 v232, v232, 9, v252\n"
            "v_lshl_add_u32 v236, v236, 9, v252\n"
            "v_lshl_add_u32 v240, v240, 9, v252\n"
            "v_lshl_add_u32 v244, v244, 9, v252\n"
            "ds_read_b32 v232, v232 offset:0\n"
            "ds_read_b32 v236, v236 offset:0\n"
            "ds_read_b32 v240, v240 offset:0\n"
            "ds_read_b32 v244, v244 offset:0\n"
            "v_lshl_add_u32 v233, v233, 9, v252\n"
            "v_lshl_add_u32 v237, v237, 9, v252\n"
            "v_lshl_add_u32 v241, v241, 9, v252\n"
            "v_lshl_add_u32 v245, v245, 9, v252\n"
            "ds_read_b32 v233, v233 offset:0\n"
            "ds_read_b32 v237, v237 offset:0\n"
            "ds_read_b32 v241, v241 offset:0\n"
            "ds_read_b32 v245, v245 offset:0\n"
            "v_lshl_add_u32 v234, v234, 9, v252\n"
            "v_lshl_add_u32 v238, v238, 9, v252\n"
            "v_lshl_add_u32 v242, v242, 9, v252\n"
            "v_lshl_add_u32 v246, v246, 9, v252\n"
            "ds_read_b32 v234, v234 offset:256\n"
            "ds_read_b32 v238, v238 offset:256\n"
            "ds_read_b32 v242, v242 offset:256\n"
            "ds_read_b32 v246, v246 offset:256\n"
            "v_lshl_add_u32 v235, v235, 9, v252\n"
            "v_lshl_add_u32 v239, v239, 9, v252\n"
            "v_lshl_add_u32 v243, v243, 9, v252\n"
            "v_lshl_add_u32 v247, v247, 9, v252\n"
            "ds_read_b32 v235, v235 offset:256\n"
            "ds_read_b32 v239, v239 offset:256\n"
            "ds_read_b32 v243, v243 offset:256\n"
            "ds_read_b32 v247, v247 offset:256\n"
            "s_waitcnt lgkmcnt(12)\n"
            "v_perm_b32 v128, v236, v232, s42\n"
            "v_perm_b32 v129, v244, v240, s42\n"
            "ds_write_b64 v248, v[128:129] offset:28672\n"
            "s_waitcnt lgkmcnt(9)\n"
            "v_perm_b32 v130, v237, v233, s43\n"
            "v_perm_b32 v131, v245, v241, s43\n"
            "ds_write_b64 v249, v[130:131] offset:28672\n"
            "s_waitcnt lgkmcnt(6)\n"
            "v_perm_b32 v132, v238, v234, s42\n"
            "v_perm_b32 v133, v246, v242, s42\n"
            "ds_write_b64 v250, v[132:133] offset:28672\n"
            "s_waitcnt lgkmcnt(3)\n"
            "v_perm_b32 v134, v239, v235, s43\n"
            "v_perm_b32 v135, v247, v243, s43\n"
            "ds_write_b64 v251, v[134:135] offset:28672\n"
            "s_waitcnt lgkmcnt(0)\n"
            "s_waitcnt lgkmcnt(0)\n"
            "v_mov_b32 v254, 1\n"
            "s_mov_b64 exec, 1\n"
            "ds_add_u32 v253, v254\n"
            "s_mov_b64 exec, -1\n"
            "8:\n"
            "ds_read_b32 v254, v253\n"
            "s_waitcnt lgkmcnt(0)\n"
            "v_cmp_ne_u32 vcc, 0x48, v254\n"
            "s_nop 1\n"
            "s_cbranch_vccnz 8b\n"
            "ds_read_b128 v[128:131], v114 offset:28672\n"
            "ds_read_b128 v[156:159], v112 offset:4096\n"
            "ds_read_b128 v[172:175], v115 offset:28672\n"
            "ds_read_b128 v[200:203], v113 offset:4096\n"
            "ds_read_b128 v[176:179], v115 offset:28928\n"
            "ds_read_b128 v[132:135], v114 offset:28928\n"
            "ds_read_b128 v[160:163], v112 offset:6144\n"
            "ds_read_b128 v[204:207], v113 offset:6144\n"
            "ds_read_b128 v[164:167], v112 offset:16384\n"
            "ds_read_b128 v[208:211], v113 offset:16384\n"
            "ds_read_b128 v[168:171], v112 offset:18432\n"
            "ds_read_b128 v[212:215], v113 offset:18432\n"
            "s_waitcnt lgkmcnt(10)\n"
            "v_mfma_f32_16x16x32_bf16 v[0:3], v[156:159], v[128:131], v[0:3]\n"
            "s_waitcnt lgkmcnt(8)\n"
            "v_mfma_f32_16x16x32_bf16 v[0:3], v[200:203], v[172:175], v[0:3]\n"
            "s_waitcnt lgkmcnt(7)\n"
            "v_mfma_f32_16x16x32_bf16 v[4:7], v[200:203], v[176:179], v[4:7]\n"
            "s_waitcnt lgkmcnt(6)\n"
            "v_mfma_f32_16x16x32_bf16 v[4:7], v[156:159], v[132:135], v[4:7]\n"
            "ds_read_b128 v[136:139], v114 offset:29184\n"
            "s_waitcnt lgkmcnt(6)\n"
            "v_mfma_f32_16x16x32_bf16 v[32:35], v[160:163], v[132:135], v[32:35]\n"
            "s_waitcnt lgkmcnt(5)\n"
            "v_mfma_f32_16x16x32_bf16 v[32:35], v[204:207], v[176:179], v[32:35]\n"
            "ds_read_b128 v[180:183], v115 offset:29184\n"
            "v_mfma_f32_16x16x32_bf16 v[28:31], v[204:207], v[172:175], v[28:31]\n"
            "v_mfma_f32_16x16x32_bf16 v[28:31], v[160:163], v[128:131], v[28:31]\n"
            "ds_read_b128 v[140:143], v114 offset:29440\n"
            "s_waitcnt lgkmcnt(3)\n"
            "v_mfma_f32_16x16x32_bf16 v[56:59], v[164:167], v[128:131], v[56:59]\n"
            "v_mfma_f32_16x16x32_bf16 v[56:59], v[208:211], v[172:175], v[56:59]\n"
            "ds_read_b128 v[184:187], v115 offset:29440\n"
            "v_mfma_f32_16x16x32_bf16 v[60:63], v[208:211], v[176:179], v[60:63]\n"
            "v_mfma_f32_16x16x32_bf16 v[60:63], v[164:167], v[132:135], v[60:63]\n"
            "ds_read_b128 v[144:147], v114 offset:29696\n"
            "v_mfma_f32_16x16x32_bf16 v[88:91], v[168:171], v[132:135], v[88:91]\n"
            "v_mfma_f32_16x16x32_bf16 v[88:91], v[212:215], v[176:179], v[88:91]\n"
            "ds_read_b128 v[188:191], v115 offset:29696\n"
            "v_mfma_f32_16x16x32_bf16 v[84:87], v[212:215], v[172:175], v[84:87]\n"
            "v_mfma_f32_16x16x32_bf16 v[84:87], v[168:171], v[128:131], v[84:87]\n"
            "ds_read_b128 v[148:151], v114 offset:29952\n"
            "s_waitcnt lgkmcnt(3)\n"
            "v_mfma_f32_16x16x32_bf16 v[92:95], v[168:171], v[136:139], v[92:95]\n"
            "v_mfma_f32_16x16x32_bf16 v[92:95], v[212:215], v[180:183], v[92:95]\n"
            "ds_read_b128 v[192:195], v115 offset:29952\n"
            "v_mfma_f32_16x16x32_bf16 v[64:67], v[208:211], v[180:183], v[64:67]\n"
            "v_mfma_f32_16x16x32_bf16 v[64:67], v[164:167], v[136:139], v[64:67]\n"
            "ds_read_b128 v[196:199], v115 offset:30208\n"
            "v_mfma_f32_16x16x32_bf16 v[36:39], v[160:163], v[136:139], v[36:39]\n"
            "v_mfma_f32_16x16x32_bf16 v[36:39], v[204:207], v[180:183], v[36:39]\n"
            "ds_read_b128 v[152:155], v114 offset:30208\n"
            "v_mfma_f32_16x16x32_bf16 v[8:11], v[200:203], v[180:183], v[8:11]\n"
            "v_mfma_f32_16x16x32_bf16 v[8:11], v[156:159], v[136:139], v[8:11]\n"
            "v_mfma_f32_16x16x32_bf16 v[12:15], v[156:159], v[140:143], v[12:15]\n"
            "v_mfma_f32_16x16x32_bf16 v[12:15], v[200:203], v[184:187], v[12:15]\n"
            "v_mfma_f32_16x16x32_bf16 v[40:43], v[204:207], v[184:187], v[40:43]\n"
            "v_mfma_f32_16x16x32_bf16 v[40:43], v[160:163], v[140:143], v[40:43]\n"
            "v_mfma_f32_16x16x32_bf16 v[68:71], v[164:167], v[140:143], v[68:71]\n"
            "v_mfma_f32_16x16x32_bf16 v[68:71], v[208:211], v[184:187], v[68:71]\n"
            "v_mfma_f32_16x16x32_bf16 v[96:99], v[212:215], v[184:187], v[96:99]\n"
            "v_mfma_f32_16x16x32_bf16 v[96:99], v[168:171], v[140:143], v[96:99]\n"
            "s_waitcnt lgkmcnt(0)\n"
            "v_mfma_f32_16x16x32_bf16 v[100:103], v[168:171], v[144:147], v[100:103]\n"
            "v_mfma_f32_16x16x32_bf16 v[100:103], v[212:215], v[188:191], v[100:103]\n"
            "v_mfma_f32_16x16x32_bf16 v[72:75], v[208:211], v[188:191], v[72:75]\n"
            "v_mfma_f32_16x16x32_bf16 v[72:75], v[164:167], v[144:147], v[72:75]\n"
            "v_mfma_f32_16x16x32_bf16 v[44:47], v[160:163], v[144:147], v[44:47]\n"
            "v_mfma_f32_16x16x32_bf16 v[44:47], v[204:207], v[188:191], v[44:47]\n"
            "v_mfma_f32_16x16x32_bf16 v[16:19], v[200:203], v[188:191], v[16:19]\n"
            "v_mfma_f32_16x16x32_bf16 v[16:19], v[156:159], v[144:147], v[16:19]\n"
            "v_mfma_f32_16x16x32_bf16 v[20:23], v[156:159], v[148:151], v[20:23]\n"
            "v_mfma_f32_16x16x32_bf16 v[20:23], v[200:203], v[192:195], v[20:23]\n"
            "v_mfma_f32_16x16x32_bf16 v[24:27], v[196:199], v[200:203], v[24:27]\n"
            "v_mfma_f32_16x16x32_bf16 v[24:27], v[152:155], v[156:159], v[24:27]\n"
            "v_mfma_f32_16x16x32_bf16 v[52:55], v[152:155], v[160:163], v[52:55]\n"
            "v_mfma_f32_16x16x32_bf16 v[52:55], v[196:199], v[204:207], v[52:55]\n"
            "v_mfma_f32_16x16x32_bf16 v[48:51], v[204:207], v[192:195], v[48:51]\n"
            "v_mfma_f32_16x16x32_bf16 v[48:51], v[160:163], v[148:151], v[48:51]\n"
            "v_mfma_f32_16x16x32_bf16 v[76:79], v[164:167], v[148:151], v[76:79]\n"
            "v_mfma_f32_16x16x32_bf16 v[76:79], v[208:211], v[192:195], v[76:79]\n"
            "v_mfma_f32_16x16x32_bf16 v[80:83], v[196:199], v[208:211], v[80:83]\n"
            "v_mfma_f32_16x16x32_bf16 v[80:83], v[152:155], v[164:167], v[80:83]\n"
            "v_mfma_f32_16x16x32_bf16 v[108:111], v[152:155], v[168:171], v[108:111]\n"
            "v_mfma_f32_16x16x32_bf16 v[108:111], v[196:199], v[212:215], v[108:111]\n"
            "v_mfma_f32_16x16x32_bf16 v[104:107], v[212:215], v[192:195], v[104:107]\n"
            "v_mfma_f32_16x16x32_bf16 v[104:107], v[168:171], v[148:151], v[104:107]\n"
            "s_waitcnt lgkmcnt(0)\n"
            "s_barrier\n"
            "ds_read_b128 v[128:131], v114 offset:57344\n"
            "ds_read_b128 v[156:159], v112 offset:8192\n"
            "ds_read_b128 v[172:175], v115 offset:57344\n"
            "ds_read_b128 v[200:203], v113 offset:8192\n"
            "ds_read_b128 v[176:179], v115 offset:57600\n"
            "ds_read_b128 v[132:135], v114 offset:57600\n"
            "ds_read_b128 v[160:163], v112 offset:10240\n"
            "ds_read_b128 v[204:207], v113 offset:10240\n"
            "ds_read_b128 v[164:167], v112 offset:20480\n"
            "ds_read_b128 v[208:211], v113 offset:20480\n"
            "ds_read_b128 v[168:171], v112 offset:22528\n"
            "ds_read_b128 v[212:215], v113 offset:22528\n"
            "s_waitcnt lgkmcnt(10)\n"
            "v_mfma_f32_16x16x32_bf16 v[0:3], v[156:159], v[128:131], v[0:3]\n"
            "s_waitcnt lgkmcnt(8)\n"
            "v_mfma_f32_16x16x32_bf16 v[0:3], v[200:203], v[172:175], v[0:3]\n"
            "s_waitcnt lgkmcnt(7)\n"
            "v_mfma_f32_16x16x32_bf16 v[4:7], v[200:203], v[176:179], v[4:7]\n"
            "s_waitcnt lgkmcnt(6)\n"
            "v_mfma_f32_16x16x32_bf16 v[4:7], v[156:159], v[132:135], v[4:7]\n"
            "ds_read_b128 v[136:139], v114 offset:57856\n"
            "s_waitcnt lgkmcnt(6)\n"
            "v_mfma_f32_16x16x32_bf16 v[32:35], v[160:163], v[132:135], v[32:35]\n"
            "s_waitcnt lgkmcnt(5)\n"
            "v_mfma_f32_16x16x32_bf16 v[32:35], v[204:207], v[176:179], v[32:35]\n"
            "ds_read_b128 v[180:183], v115 offset:57856\n"
            "v_mfma_f32_16x16x32_bf16 v[28:31], v[204:207], v[172:175], v[28:31]\n"
            "v_mfma_f32_16x16x32_bf16 v[28:31], v[160:163], v[128:131], v[28:31]\n"
            "ds_read_b128 v[140:143], v114 offset:58112\n"
            "s_waitcnt lgkmcnt(3)\n"
            "v_mfma_f32_16x16x32_bf16 v[56:59], v[164:167], v[128:131], v[56:59]\n"
            "v_mfma_f32_16x16x32_bf16 v[56:59], v[208:211], v[172:175], v[56:59]\n"
            "ds_read_b128 v[184:187], v115 offset:58112\n"
            "v_mfma_f32_16x16x32_bf16 v[60:63], v[208:211], v[176:179], v[60:63]\n"
            "v_mfma_f32_16x16x32_bf16 v[60:63], v[164:167], v[132:135], v[60:63]\n"
            "ds_read_b128 v[144:147], v114 offset:58368\n"
            "v_mfma_f32_16x16x32_bf16 v[88:91], v[168:171], v[132:135], v[88:91]\n"
            "v_mfma_f32_16x16x32_bf16 v[88:91], v[212:215], v[176:179], v[88:91]\n"
            "ds_read_b128 v[188:191], v115 offset:58368\n"
            "v_mfma_f32_16x16x32_bf16 v[84:87], v[212:215], v[172:175], v[84:87]\n"
            "v_mfma_f32_16x16x32_bf16 v[84:87], v[168:171], v[128:131], v[84:87]\n"
            "ds_read_b128 v[148:151], v114 offset:58624\n"
            "s_waitcnt lgkmcnt(3)\n"
            "v_mfma_f32_16x16x32_bf16 v[92:95], v[168:171], v[136:139], v[92:95]\n"
            "v_mfma_f32_16x16x32_bf16 v[92:95], v[212:215], v[180:183], v[92:95]\n"
            "ds_read_b128 v[192:195], v115 offset:58624\n"
            "v_mfma_f32_16x16x32_bf16 v[64:67], v[208:211], v[180:183], v[64:67]\n"
            "v_mfma_f32_16x16x32_bf16 v[64:67], v[164:167], v[136:139], v[64:67]\n"
            "ds_read_b128 v[196:199], v115 offset:58880\n"
            "v_mfma_f32_16x16x32_bf16 v[36:39], v[160:163], v[136:139], v[36:39]\n"
            "v_mfma_f32_16x16x32_bf16 v[36:39], v[204:207], v[180:183], v[36:39]\n"
            "ds_read_b128 v[152:155], v114 offset:58880\n"
            "v_mfma_f32_16x16x32_bf16 v[8:11], v[200:203], v[180:183], v[8:11]\n"
            "v_mfma_f32_16x16x32_bf16 v[8:11], v[156:159], v[136:139], v[8:11]\n"
            "v_mfma_f32_16x16x32_bf16 v[12:15], v[156:159], v[140:143], v[12:15]\n"
            "v_mfma_f32_16x16x32_bf16 v[12:15], v[200:203], v[184:187], v[12:15]\n"
            "v_mfma_f32_16x16x32_bf16 v[40:43], v[204:207], v[184:187], v[40:43]\n"
            "v_mfma_f32_16x16x32_bf16 v[40:43], v[160:163], v[140:143], v[40:43]\n"
            "s_mov_b32 s41, 3\n"
            "1:\n"
            "s_waitcnt lgkmcnt(0)\n"
            "s_barrier\n"
            "v_mfma_f32_16x16x32_bf16 v[68:71], v[164:167], v[140:143], v[68:71]\n"
            "v_mfma_f32_16x16x32_bf16 v[68:71], v[208:211], v[184:187], v[68:71]\n"
            "ds_read_b128 v[128:131], v114 offset:0\n"
            "v_mfma_f32_16x16x32_bf16 v[96:99], v[212:215], v[184:187], v[96:99]\n"
            "v_mfma_f32_16x16x32_bf16 v[96:99], v[168:171], v[140:143], v[96:99]\n"
            "ds_read_b128 v[172:175], v115 offset:0\n"
            "v_mfma_f32_16x16x32_bf16 v[100:103], v[168:171], v[144:147], v[100:103]\n"
            "v_mfma_f32_16x16x32_bf16 v[100:103], v[212:215], v[188:191], v[100:103]\n"
            "ds_read_b128 v[176:179], v115 offset:256\n"
            "v_mfma_f32_16x16x32_bf16 v[72:75], v[208:211], v[188:191], v[72:75]\n"
            "v_mfma_f32_16x16x32_bf16 v[72:75], v[164:167], v[144:147], v[72:75]\n"
            "ds_read_b128 v[132:135], v114 offset:256\n"
            "v_mfma_f32_16x16x32_bf16 v[44:47], v[160:163], v[144:147], v[44:47]\n"
            "v_mfma_f32_16x16x32_bf16 v[44:47], v[204:207], v[188:191], v[44:47]\n"
            "v_mfma_f32_16x16x32_bf16 v[16:19], v[200:203], v[188:191], v[16:19]\n"
            "v_mfma_f32_16x16x32_bf16 v[16:19], v[156:159], v[144:147], v[16:19]\n"
            "v_mfma_f32_16x16x32_bf16 v[20:23], v[156:159], v[148:151], v[20:23]\n"
            "v_mfma_f32_16x16x32_bf16 v[20:23], v[200:203], v[192:195], v[20:23]\n"
            "v_mfma_f32_16x16x32_bf16 v[24:27], v[196:199], v[200:203], v[24:27]\n"
            "v_mfma_f32_16x16x32_bf16 v[24:27], v[152:155], v[156:159], v[24:27]\n"
            "ds_read_b128 v[156:159], v112 offset:0\n"
            "v_mfma_f32_16x16x32_bf16 v[52:55], v[152:155], v[160:163], v[52:55]\n"
            "v_mfma_f32_16x16x32_bf16 v[52:55], v[196:199], v[204:207], v[52:55]\n"
            "ds_read_b128 v[200:203], v113 offset:0\n"
            "v_mfma_f32_16x16x32_bf16 v[48:51], v[204:207], v[192:195], v[48:51]\n"
            "v_mfma_f32_16x16x32_bf16 v[48:51], v[160:163], v[148:151], v[48:51]\n"
            "ds_read_b128 v[160:163], v112 offset:2048\n"
            "v_mfma_f32_16x16x32_bf16 v[76:79], v[164:167], v[148:151], v[76:79]\n"
            "v_mfma_f32_16x16x32_bf16 v[76:79], v[208:211], v[192:195], v[76:79]\n"
            "ds_read_b128 v[204:207], v113 offset:2048\n"
            "v_mfma_f32_16x16x32_bf16 v[80:83], v[196:199], v[208:211], v[80:83]\n"
            "v_mfma_f32_16x16x32_bf16 v[80:83], v[152:155], v[164:167], v[80:83]\n"
            "ds_read_b128 v[164:167], v112 offset:12288\n"
            "v_mfma_f32_16x16x32_bf16 v[108:111], v[152:155], v[168:171], v[108:111]\n"
            "v_mfma_f32_16x16x32_bf16 v[108:111], v[196:199], v[212:215], v[108:111]\n"
            "ds_read_b128 v[208:211], v113 offset:12288\n"
            "v_mfma_f32_16x16x32_bf16 v[104:107], v[212:215], v[192:195], v[104:107]\n"
            "v_mfma_f32_16x16x32_bf16 v[104:107], v[168:171], v[148:151], v[104:107]\n"
            "ds_read_b128 v[168:171], v112 offset:14336\n"
            "s_sub_u32 s41, s41, 1\n"
            "s_cbranch_scc1 2f\n"
            "s_waitcnt lgkmcnt(3)\n"
            "v_mfma_f32_16x16x32_bf16 v[0:3], v[156:159], v[128:131], v[0:3]\n"
            "v_mfma_f32_16x16x32_bf16 v[0:3], v[200:203], v[172:175], v[0:3]\n"
            "ds_read_b128 v[212:215], v113 offset:14336\n"
            "v_mfma_f32_16x16x32_bf16 v[4:7], v[200:203], v[176:179], v[4:7]\n"
            "v_mfma_f32_16x16x32_bf16 v[4:7], v[156:159], v[132:135], v[4:7]\n"
            "ds_read_b128 v[136:139], v114 offset:512\n"
            "v_mfma_f32_16x16x32_bf16 v[32:35], v[160:163], v[132:135], v[32:35]\n"
            "v_mfma_f32_16x16x32_bf16 v[32:35], v[204:207], v[176:179], v[32:35]\n"
            "ds_read_b128 v[180:183], v115 offset:512\n"
            "v_mfma_f32_16x16x32_bf16 v[28:31], v[204:207], v[172:175], v[28:31]\n"
            "v_mfma_f32_16x16x32_bf16 v[28:31], v[160:163], v[128:131], v[28:31]\n"
            "ds_read_b128 v[140:143], v114 offset:768\n"
            "s_waitcnt lgkmcnt(3)\n"
            "v_mfma_f32_16x16x32_bf16 v[56:59], v[164:167], v[128:131], v[56:59]\n"
            "v_mfma_f32_16x16x32_bf16 v[56:59], v[208:211], v[172:175], v[56:59]\n"
            "ds_read_b128 v[184:187], v115 offset:768\n"
            "v_mfma_f32_16x16x32_bf16 v[60:63], v[208:211], v[176:179], v[60:63]\n"
            "v_mfma_f32_16x16x32_bf16 v[60:63], v[164:167], v[132:135], v[60:63]\n"
            "ds_read_b128 v[144:147], v114 offset:1024\n"
            "v_mfma_f32_16x16x32_bf16 v[88:91], v[168:171], v[132:135], v[88:91]\n"
            "v_mfma_f32_16x16x32_bf16 v[88:91], v[212:215], v[176:179], v[88:91]\n"
            "ds_read_b128 v[188:191], v115 offset:1024\n"
            "v_mfma_f32_16x16x32_bf16 v[84:87], v[212:215], v[172:175], v[84:87]\n"
            "v_mfma_f32_16x16x32_bf16 v[84:87], v[168:171], v[128:131], v[84:87]\n"
            "ds_read_b128 v[148:151], v114 offset:1280\n"
            "s_waitcnt lgkmcnt(3)\n"
            "v_mfma_f32_16x16x32_bf16 v[92:95], v[168:171], v[136:139], v[92:95]\n"
            "v_mfma_f32_16x16x32_bf16 v[92:95], v[212:215], v[180:183], v[92:95]\n"
            "ds_read_b128 v[192:195], v115 offset:1280\n"
            "v_mfma_f32_16x16x32_bf16 v[64:67], v[208:211], v[180:183], v[64:67]\n"
            "v_mfma_f32_16x16x32_bf16 v[64:67], v[164:167], v[136:139], v[64:67]\n"
            "ds_read_b128 v[196:199], v115 offset:1536\n"
            "v_mfma_f32_16x16x32_bf16 v[36:39], v[160:163], v[136:139], v[36:39]\n"
            "v_mfma_f32_16x16x32_bf16 v[36:39], v[204:207], v[180:183], v[36:39]\n"
            "ds_read_b128 v[152:155], v114 offset:1536\n"
            "v_mfma_f32_16x16x32_bf16 v[8:11], v[200:203], v[180:183], v[8:11]\n"
            "v_mfma_f32_16x16x32_bf16 v[8:11], v[156:159], v[136:139], v[8:11]\n"
            "v_mfma_f32_16x16x32_bf16 v[12:15], v[156:159], v[140:143], v[12:15]\n"
            "v_mfma_f32_16x16x32_bf16 v[12:15], v[200:203], v[184:187], v[12:15]\n"
            "v_mfma_f32_16x16x32_bf16 v[40:43], v[204:207], v[184:187], v[40:43]\n"
            "v_mfma_f32_16x16x32_bf16 v[40:43], v[160:163], v[140:143], v[40:43]\n"
            "s_waitcnt lgkmcnt(0)\n"
            "s_barrier\n"
            "v_mfma_f32_16x16x32_bf16 v[68:71], v[164:167], v[140:143], v[68:71]\n"
            "v_mfma_f32_16x16x32_bf16 v[68:71], v[208:211], v[184:187], v[68:71]\n"
            "ds_read_b128 v[128:131], v114 offset:28672\n"
            "v_mfma_f32_16x16x32_bf16 v[96:99], v[212:215], v[184:187], v[96:99]\n"
            "v_mfma_f32_16x16x32_bf16 v[96:99], v[168:171], v[140:143], v[96:99]\n"
            "ds_read_b128 v[172:175], v115 offset:28672\n"
            "v_mfma_f32_16x16x32_bf16 v[100:103], v[168:171], v[144:147], v[100:103]\n"
            "v_mfma_f32_16x16x32_bf16 v[100:103], v[212:215], v[188:191], v[100:103]\n"
            "ds_read_b128 v[176:179], v115 offset:28928\n"
            "v_mfma_f32_16x16x32_bf16 v[72:75], v[208:211], v[188:191], v[72:75]\n"
            "v_mfma_f32_16x16x32_bf16 v[72:75], v[164:167], v[144:147], v[72:75]\n"
            "ds_read_b128 v[132:135], v114 offset:28928\n"
            "v_mfma_f32_16x16x32_bf16 v[44:47], v[160:163], v[144:147], v[44:47]\n"
            "v_mfma_f32_16x16x32_bf16 v[44:47], v[204:207], v[188:191], v[44:47]\n"
            "v_mfma_f32_16x16x32_bf16 v[16:19], v[200:203], v[188:191], v[16:19]\n"
            "v_mfma_f32_16x16x32_bf16 v[16:19], v[156:159], v[144:147], v[16:19]\n"
            "v_mfma_f32_16x16x32_bf16 v[20:23], v[156:159], v[148:151], v[20:23]\n"
            "v_mfma_f32_16x16x32_bf16 v[20:23], v[200:203], v[192:195], v[20:23]\n"
            "v_mfma_f32_16x16x32_bf16 v[24:27], v[196:199], v[200:203], v[24:27]\n"
            "v_mfma_f32_16x16x32_bf16 v[24:27], v[152:155], v[156:159], v[24:27]\n"
            "ds_read_b128 v[156:159], v112 offset:4096\n"
            "v_mfma_f32_16x16x32_bf16 v[52:55], v[152:155], v[160:163], v[52:55]\n"
            "v_mfma_f32_16x16x32_bf16 v[52:55], v[196:199], v[204:207], v[52:55]\n"
            "ds_read_b128 v[200:203], v113 offset:4096\n"
            "v_mfma_f32_16x16x32_bf16 v[48:51], v[204:207], v[192:195], v[48:51]\n"
            "v_mfma_f32_16x16x32_bf16 v[48:51], v[160:163], v[148:151], v[48:51]\n"
            "ds_read_b128 v[160:163], v112 offset:6144\n"
            "v_mfma_f32_16x16x32_bf16 v[76:79], v[164:167], v[148:151], v[76:79]\n"
            "v_mfma_f32_16x16x32_bf16 v[76:79], v[208:211], v[192:195], v[76:79]\n"
            "ds_read_b128 v[204:207], v113 offset:6144\n"
            "v_mfma_f32_16x16x32_bf16 v[80:83], v[196:199], v[208:211], v[80:83]\n"
            "v_mfma_f32_16x16x32_bf16 v[80:83], v[152:155], v[164:167], v[80:83]\n"
            "ds_read_b128 v[164:167], v112 offset:16384\n"
            "v_mfma_f32_16x16x32_bf16 v[108:111], v[152:155], v[168:171], v[108:111]\n"
            "v_mfma_f32_16x16x32_bf16 v[108:111], v[196:199], v[212:215], v[108:111]\n"
            "ds_read_b128 v[208:211], v113 offset:16384\n"
            "v_mfma_f32_16x16x32_bf16 v[104:107], v[212:215], v[192:195], v[104:107]\n"
            "v_mfma_f32_16x16x32_bf16 v[104:107], v[168:171], v[148:151], v[104:107]\n"
            "ds_read_b128 v[168:171], v112 offset:18432\n"
            "s_waitcnt lgkmcnt(3)\n"
            "v_mfma_f32_16x16x32_bf16 v[0:3], v[156:159], v[128:131], v[0:3]\n"
            "v_mfma_f32_16x16x32_bf16 v[0:3], v[200:203], v[172:175], v[0:3]\n"
            "ds_read_b128 v[212:215], v113 offset:18432\n"
            "v_mfma_f32_16x16x32_bf16 v[4:7], v[200:203], v[176:179], v[4:7]\n"
            "v_mfma_f32_16x16x32_bf16 v[4:7], v[156:159], v[132:135], v[4:7]\n"
            "ds_read_b128 v[136:139], v114 offset:29184\n"
            "v_mfma_f32_16x16x32_bf16 v[32:35], v[160:163], v[132:135], v[32:35]\n"
            "v_mfma_f32_16x16x32_bf16 v[32:35], v[204:207], v[176:179], v[32:35]\n"
            "ds_read_b128 v[180:183], v115 offset:29184\n"
            "v_mfma_f32_16x16x32_bf16 v[28:31], v[204:207], v[172:175], v[28:31]\n"
            "v_mfma_f32_16x16x32_bf16 v[28:31], v[160:163], v[128:131], v[28:31]\n"
            "ds_read_b128 v[140:143], v114 offset:29440\n"
            "s_waitcnt lgkmcnt(3)\n"
            "v_mfma_f32_16x16x32_bf16 v[56:59], v[164:167], v[128:131], v[56:59]\n"
            "v_mfma_f32_16x16x32_bf16 v[56:59], v[208:211], v[172:175], v[56:59]\n"
            "ds_read_b128 v[184:187], v115 offset:29440\n"
            "v_mfma_f32_16x16x32_bf16 v[60:63], v[208:211], v[176:179], v[60:63]\n"
            "v_mfma_f32_16x16x32_bf16 v[60:63], v[164:167], v[132:135], v[60:63]\n"
            "ds_read_b128 v[144:147], v114 offset:29696\n"
            "v_mfma_f32_16x16x32_bf16 v[88:91], v[168:171], v[132:135], v[88:91]\n"
            "v_mfma_f32_16x16x32_bf16 v[88:91], v[212:215], v[176:179], v[88:91]\n"
            "ds_read_b128 v[188:191], v115 offset:29696\n"
            "v_mfma_f32_16x16x32_bf16 v[84:87], v[212:215], v[172:175], v[84:87]\n"
            "v_mfma_f32_16x16x32_bf16 v[84:87], v[168:171], v[128:131], v[84:87]\n"
            "ds_read_b128 v[148:151], v114 offset:29952\n"
            "s_waitcnt lgkmcnt(3)\n"
            "v_mfma_f32_16x16x32_bf16 v[92:95], v[168:171], v[136:139], v[92:95]\n"
            "v_mfma_f32_16x16x32_bf16 v[92:95], v[212:215], v[180:183], v[92:95]\n"
            "ds_read_b128 v[192:195], v115 offset:29952\n"
            "v_mfma_f32_16x16x32_bf16 v[64:67], v[208:211], v[180:183], v[64:67]\n"
            "v_mfma_f32_16x16x32_bf16 v[64:67], v[164:167], v[136:139], v[64:67]\n"
            "ds_read_b128 v[196:199], v115 offset:30208\n"
            "v_mfma_f32_16x16x32_bf16 v[36:39], v[160:163], v[136:139], v[36:39]\n"
            "v_mfma_f32_16x16x32_bf16 v[36:39], v[204:207], v[180:183], v[36:39]\n"
            "ds_read_b128 v[152:155], v114 offset:30208\n"
            "v_mfma_f32_16x16x32_bf16 v[8:11], v[200:203], v[180:183], v[8:11]\n"
            "v_mfma_f32_16x16x32_bf16 v[8:11], v[156:159], v[136:139], v[8:11]\n"
            "v_mfma_f32_16x16x32_bf16 v[12:15], v[156:159], v[140:143], v[12:15]\n"
            "v_mfma_f32_16x16x32_bf16 v[12:15], v[200:203], v[184:187], v[12:15]\n"
            "v_mfma_f32_16x16x32_bf16 v[40:43], v[204:207], v[184:187], v[40:43]\n"
            "v_mfma_f32_16x16x32_bf16 v[40:43], v[160:163], v[140:143], v[40:43]\n"
            "s_waitcnt lgkmcnt(0)\n"
            "s_barrier\n"
            "v_mfma_f32_16x16x32_bf16 v[68:71], v[164:167], v[140:143], v[68:71]\n"
            "v_mfma_f32_16x16x32_bf16 v[68:71], v[208:211], v[184:187], v[68:71]\n"
            "ds_read_b128 v[128:131], v114 offset:57344\n"
            "v_mfma_f32_16x16x32_bf16 v[96:99], v[212:215], v[184:187], v[96:99]\n"
            "v_mfma_f32_16x16x32_bf16 v[96:99], v[168:171], v[140:143], v[96:99]\n"
            "ds_read_b128 v[172:175], v115 offset:57344\n"
            "v_mfma_f32_16x16x32_bf16 v[100:103], v[168:171], v[144:147], v[100:103]\n"
            "v_mfma_f32_16x16x32_bf16 v[100:103], v[212:215], v[188:191], v[100:103]\n"
            "ds_read_b128 v[176:179], v115 offset:57600\n"
            "v_mfma_f32_16x16x32_bf16 v[72:75], v[208:211], v[188:191], v[72:75]\n"
            "v_mfma_f32_16x16x32_bf16 v[72:75], v[164:167], v[144:147], v[72:75]\n"
            "ds_read_b128 v[132:135], v114 offset:57600\n"
            "v_mfma_f32_16x16x32_bf16 v[44:47], v[160:163], v[144:147], v[44:47]\n"
            "v_mfma_f32_16x16x32_bf16 v[44:47], v[204:207], v[188:191], v[44:47]\n"
            "v_mfma_f32_16x16x32_bf16 v[16:19], v[200:203], v[188:191], v[16:19]\n"
            "v_mfma_f32_16x16x32_bf16 v[16:19], v[156:159], v[144:147], v[16:19]\n"
            "v_mfma_f32_16x16x32_bf16 v[20:23], v[156:159], v[148:151], v[20:23]\n"
            "v_mfma_f32_16x16x32_bf16 v[20:23], v[200:203], v[192:195], v[20:23]\n"
            "v_mfma_f32_16x16x32_bf16 v[24:27], v[196:199], v[200:203], v[24:27]\n"
            "v_mfma_f32_16x16x32_bf16 v[24:27], v[152:155], v[156:159], v[24:27]\n"
            "ds_read_b128 v[156:159], v112 offset:8192\n"
            "v_mfma_f32_16x16x32_bf16 v[52:55], v[152:155], v[160:163], v[52:55]\n"
            "v_mfma_f32_16x16x32_bf16 v[52:55], v[196:199], v[204:207], v[52:55]\n"
            "ds_read_b128 v[200:203], v113 offset:8192\n"
            "v_mfma_f32_16x16x32_bf16 v[48:51], v[204:207], v[192:195], v[48:51]\n"
            "v_mfma_f32_16x16x32_bf16 v[48:51], v[160:163], v[148:151], v[48:51]\n"
            "ds_read_b128 v[160:163], v112 offset:10240\n"
            "v_mfma_f32_16x16x32_bf16 v[76:79], v[164:167], v[148:151], v[76:79]\n"
            "v_mfma_f32_16x16x32_bf16 v[76:79], v[208:211], v[192:195], v[76:79]\n"
            "ds_read_b128 v[204:207], v113 offset:10240\n"
            "v_mfma_f32_16x16x32_bf16 v[80:83], v[196:199], v[208:211], v[80:83]\n"
            "v_mfma_f32_16x16x32_bf16 v[80:83], v[152:155], v[164:167], v[80:83]\n"
            "ds_read_b128 v[164:167], v112 offset:20480\n"
            "v_mfma_f32_16x16x32_bf16 v[108:111], v[152:155], v[168:171], v[108:111]\n"
            "v_mfma_f32_16x16x32_bf16 v[108:111], v[196:199], v[212:215], v[108:111]\n"
            "ds_read_b128 v[208:211], v113 offset:20480\n"
            "v_mfma_f32_16x16x32_bf16 v[104:107], v[212:215], v[192:195], v[104:107]\n"
            "v_mfma_f32_16x16x32_bf16 v[104:107], v[168:171], v[148:151], v[104:107]\n"
            "ds_read_b128 v[168:171], v112 offset:22528\n"
            "s_waitcnt lgkmcnt(3)\n"
            "v_mfma_f32_16x16x32_bf16 v[0:3], v[156:159], v[128:131], v[0:3]\n"
            "v_mfma_f32_16x16x32_bf16 v[0:3], v[200:203], v[172:175], v[0:3]\n"
            "ds_read_b128 v[212:215], v113 offset:22528\n"
            "v_mfma_f32_16x16x32_bf16 v[4:7], v[200:203], v[176:179], v[4:7]\n"
            "v_mfma_f32_16x16x32_bf16 v[4:7], v[156:159], v[132:135], v[4:7]\n"
            "ds_read_b128 v[136:139], v114 offset:57856\n"
            "v_mfma_f32_16x16x32_bf16 v[32:35], v[160:163], v[132:135], v[32:35]\n"
            "v_mfma_f32_16x16x32_bf16 v[32:35], v[204:207], v[176:179], v[32:35]\n"
            "ds_read_b128 v[180:183], v115 offset:57856\n"
            "v_mfma_f32_16x16x32_bf16 v[28:31], v[204:207], v[172:175], v[28:31]\n"
            "v_mfma_f32_16x16x32_bf16 v[28:31], v[160:163], v[128:131], v[28:31]\n"
            "ds_read_b128 v[140:143], v114 offset:58112\n"
            "s_waitcnt lgkmcnt(3)\n"
            "v_mfma_f32_16x16x32_bf16 v[56:59], v[164:167], v[128:131], v[56:59]\n"
            "v_mfma_f32_16x16x32_bf16 v[56:59], v[208:211], v[172:175], v[56:59]\n"
            "ds_read_b128 v[184:187], v115 offset:58112\n"
            "v_mfma_f32_16x16x32_bf16 v[60:63], v[208:211], v[176:179], v[60:63]\n"
            "v_mfma_f32_16x16x32_bf16 v[60:63], v[164:167], v[132:135], v[60:63]\n"
            "ds_read_b128 v[144:147], v114 offset:58368\n"
            "v_mfma_f32_16x16x32_bf16 v[88:91], v[168:171], v[132:135], v[88:91]\n"
            "v_mfma_f32_16x16x32_bf16 v[88:91], v[212:215], v[176:179], v[88:91]\n"
            "ds_read_b128 v[188:191], v115 offset:58368\n"
            "v_mfma_f32_16x16x32_bf16 v[84:87], v[212:215], v[172:175], v[84:87]\n"
            "v_mfma_f32_16x16x32_bf16 v[84:87], v[168:171], v[128:131], v[84:87]\n"
            "ds_read_b128 v[148:151], v114 offset:58624\n"
            "s_waitcnt lgkmcnt(3)\n"
            "v_mfma_f32_16x16x32_bf16 v[92:95], v[168:171], v[136:139], v[92:95]\n"
            "v_mfma_f32_16x16x32_bf16 v[92:95], v[212:215], v[180:183], v[92:95]\n"
            "ds_read_b128 v[192:195], v115 offset:58624\n"
            "v_mfma_f32_16x16x32_bf16 v[64:67], v[208:211], v[180:183], v[64:67]\n"
            "v_mfma_f32_16x16x32_bf16 v[64:67], v[164:167], v[136:139], v[64:67]\n"
            "ds_read_b128 v[196:199], v115 offset:58880\n"
            "v_mfma_f32_16x16x32_bf16 v[36:39], v[160:163], v[136:139], v[36:39]\n"
            "v_mfma_f32_16x16x32_bf16 v[36:39], v[204:207], v[180:183], v[36:39]\n"
            "ds_read_b128 v[152:155], v114 offset:58880\n"
            "v_mfma_f32_16x16x32_bf16 v[8:11], v[200:203], v[180:183], v[8:11]\n"
            "v_mfma_f32_16x16x32_bf16 v[8:11], v[156:159], v[136:139], v[8:11]\n"
            "v_mfma_f32_16x16x32_bf16 v[12:15], v[156:159], v[140:143], v[12:15]\n"
            "v_mfma_f32_16x16x32_bf16 v[12:15], v[200:203], v[184:187], v[12:15]\n"
            "v_mfma_f32_16x16x32_bf16 v[40:43], v[204:207], v[184:187], v[40:43]\n"
            "v_mfma_f32_16x16x32_bf16 v[40:43], v[160:163], v[140:143], v[40:43]\n"
            "s_branch 1b\n"
            ".p2align 12\n"
            "sq_pg3:\n"
            "s_setpc_b64 s[60:61]\n"
            "2:\n"
            "s_waitcnt lgkmcnt(3)\n"
            "v_mfma_f32_16x16x32_bf16 v[0:3], v[156:159], v[128:131], v[0:3]\n"
            "ds_read_b128 v[216:219], v113 offset:14336\n"
            "v_mfma_f32_16x16x32_bf16 v[0:3], v[200:203], v[172:175], v[0:3]\n"
            "v_mfma_f32_16x16x32_bf16 v[4:7], v[200:203], v[176:179], v[4:7]\n"
            "v_mfma_f32_16x16x32_bf16 v[4:7], v[156:159], v[132:135], v[4:7]\n"
            "v_mfma_f32_16x16x32_bf16 v[32:35], v[160:163], v[132:135], v[32:35]\n"
            "s_waitcnt lgkmcnt(0)\n"
            "s_barrier\n"
            "v_mfma_f32_16x16x32_bf16 v[32:35], v[204:207], v[176:179], v[32:35]\n"
            "ds_read_b128 v[220:223], v120 offset:0\n"
            "ds_read_b128 v[224:227], v112 offset:4096\n"
            "v_mfma_f32_16x16x32_bf16 v[28:31], v[204:207], v[172:175], v[28:31]\n"
            "ds_read_b128 v[228:231], v120 offset:2048\n"
            "ds_read_b128 v[232:235], v113 offset:4096\n"
            "v_mfma_f32_16x16x32_bf16 v[28:31], v[160:163], v[128:131], v[28:31]\n"
            "ds_read_b128 v[236:239], v120 offset:8192\n"
            "ds_read_b128 v[240:243], v118 offset:0\n"
            "v_mfma_f32_16x16x32_bf16 v[56:59], v[164:167], v[128:131], v[56:59]\n"
            "ds_read_b128 v[244:247], v120 offset:10240\n"
            "ds_read_b128 v[248:251], v119 offset:0\n"
            "v_mfma_f32_16x16x32_bf16 v[56:59], v[208:211], v[172:175], v[56:59]\n"
            "ds_read_b128 v[252:255], v116 offset:45056\n"
            "ds_read_b128 v[136:139], v118 offset:45056\n"
            "v_mfma_f32_16x16x32_bf16 v[60:63], v[208:211], v[176:179], v[60:63]\n"
            "ds_read_b128 v[140:143], v116 offset:47104\n"
            "ds_read_b128 v[144:147], v119 offset:45056\n"
            "v_mfma_f32_16x16x32_bf16 v[60:63], v[164:167], v[132:135], v[60:63]\n"
            "ds_read_b128 v[148:151], v116 offset:51200\n"
            "ds_read_b128 v[152:155], v116 offset:49152\n"
            "v_mfma_f32_16x16x32_bf16 v[88:91], v[168:171], v[132:135], v[88:91]\n"
            "ds_read_b128 v[132:135], v116 offset:55296\n"
            "ds_read_b128 v[180:183], v116 offset:53248\n"
            "v_mfma_f32_16x16x32_bf16 v[88:91], v[216:219], v[176:179], v[88:91]\n"
            "ds_read_b128 v[176:179], v120 offset:6144\n"
            "ds_read_b128 v[184:187], v120 offset:4096\n"
            "v_mfma_f32_16x16x32_bf16 v[84:87], v[216:219], v[172:175], v[84:87]\n"
            "ds_read_b128 v[172:175], v112 offset:6144\n"
            "ds_read_b128 v[188:191], v113 offset:6144\n"
            "v_mfma_f32_16x16x32_bf16 v[84:87], v[168:171], v[128:131], v[84:87]\n"
            "ds_read_b128 v[128:131], v118 offset:2048\n"
            "s_waitcnt lgkmcnt(11)\n"
            "v_mfma_f32_16x16x32_bf16 v[0:3], v[224:227], v[220:223], v[0:3]\n"
            "ds_read_b128 v[192:195], v119 offset:2048\n"
            "v_mfma_f32_16x16x32_bf16 v[0:3], v[232:235], v[228:231], v[0:3]\n"
            "ds_read_b128 v[196:199], v118 offset:47104\n"
            "v_mfma_f32_16x16x32_bf16 v[0:3], v[240:243], v[236:239], v[0:3]\n"
            "ds_read_b128 v[212:215], v119 offset:47104\n"
            "v_mfma_f32_16x16x32_bf16 v[0:3], v[248:251], v[244:247], v[0:3]\n"
            "v_mfma_f32_16x16x32_bf16 v[0:3], v[136:139], v[252:255], v[0:3]\n"
            "s_waitcnt lgkmcnt(4)\n"
            "v_mfma_f32_16x16x32_bf16 v[0:3], v[144:147], v[140:143], v[0:3]\n"
            "v_mfma_f32_16x16x32_bf16 v[4:7], v[144:147], v[148:151], v[4:7]\n"
            "ds_read_b128 v[144:147], v112 offset:16384\n"
            "v_mfma_f32_16x16x32_bf16 v[4:7], v[136:139], v[152:155], v[4:7]\n"
            "ds_read_b128 v[136:139], v113 offset:16384\n"
            "v_mfma_f32_16x16x32_bf16 v[4:7], v[248:251], v[132:135], v[4:7]\n"
            "ds_read_b128 v[248:251], v118 offset:4096\n"
            "v_mfma_f32_16x16x32_bf16 v[4:7], v[240:243], v[180:183], v[4:7]\n"
            "ds_read_b128 v[240:243], v119 offset:4096\n"
            "v_mfma_f32_16x16x32_bf16 v[4:7], v[232:235], v[176:179], v[4:7]\n"
            "ds_read_b128 v[232:235], v118 offset:49152\n"
            "v_mfma_f32_16x16x32_bf16 v[4:7], v[224:227], v[184:187], v[4:7]\n"
            "ds_read_b128 v[224:227], v119 offset:49152\n"
            "s_nop 0\n"
            "v_mfma_f32_16x16x32_bf16 v[32:35], v[172:175], v[184:187], v[32:35]\n"
            "s_nop 1\n"
            "v_mfma_f32_16x16x32_bf16 v[32:35], v[188:191], v[176:179], v[32:35]\n"
            "s_nop 0\n"
            "s_waitcnt lgkmcnt(4)\n"
            "v_mfma_f32_16x16x32_bf16 v[32:35], v[128:131], v[180:183], v[32:35]\n"
            "v_cvt_pk_bf16_f32 v0, v0, v4\n"
            "v_cvt_pk_bf16_f32 v1, v1, v5\n"
            "v_mfma_f32_16x16x32_bf16 v[32:35], v[192:195], v[132:135], v[32:35]\n"
            "global_store_dword v124, v0, s[64:65] offset:0 sc1\n"
            "v_cvt_pk_bf16_f32 v2, v2, v6\n"
            "v_mfma_f32_16x16x32_bf16 v[32:35], v[196:199], v[152:155], v[32:35]\n"
            "v_cvt_pk_bf16_f32 v3, v3, v7\n"
            "v_mfma_f32_16x16x32_bf16 v[32:35], v[212:215], v[148:151], v[32:35]\n"
            "global_store_dword v125, v1, s[64:65] offset:0 sc1\n"
            "v_mfma_f32_16x16x32_bf16 v[28:31], v[212:215], v[140:143], v[28:31]\n"
            "ds_read_b128 v[212:215], v112 offset:18432\n"
            "v_mfma_f32_16x16x32_bf16 v[28:31], v[196:199], v[252:255], v[28:31]\n"
            "ds_read_b128 v[196:199], v113 offset:18432\n"
            "v_mfma_f32_16x16x32_bf16 v[28:31], v[192:195], v[244:247], v[28:31]\n"
            "global_store_dword v126, v2, s[64:65] offset:0 sc1\n"
            "v_mfma_f32_16x16x32_bf16 v[28:31], v[128:131], v[236:239], v[28:31]\n"
            "ds_read_b128 v[128:131], v118 offset:6144\n"
            "v_mfma_f32_16x16x32_bf16 v[28:31], v[188:191], v[228:231], v[28:31]\n"
            "ds_read_b128 v[188:191], v119 offset:6144\n"
            "v_mfma_f32_16x16x32_bf16 v[28:31], v[172:175], v[220:223], v[28:31]\n"
            "global_store_dword v127, v3, s[64:65] offset:0 sc1\n"
            "s_nop 0\n"
            "v_mfma_f32_16x16x32_bf16 v[56:59], v[144:147], v[220:223], v[56:59]\n"
            "ds_read_b128 v[172:175], v118 offset:51200\n"
            "s_nop 0\n"
            "v_mfma_f32_16x16x32_bf16 v[56:59], v[136:139], v[228:231], v[56:59]\n"
            "ds_read_b128 v[192:195], v119 offset:51200\n"
            "s_waitcnt lgkmcnt(4)\n"
            "v_mfma_f32_16x16x32_bf16 v[56:59], v[248:251], v[236:239], v[56:59]\n"
            "ds_read_b128 v[4:7], v114 offset:512\n"
            "v_cvt_pk_bf16_f32 v28, v28, v32\n"
            "v_mfma_f32_16x16x32_bf16 v[56:59], v[240:243], v[244:247], v[56:59]\n"
            "global_store_dword v124, v28, s[66:67] offset:0 sc1\n"
            "v_cvt_pk_bf16_f32 v29, v29, v33\n"
            "v_mfma_f32_16x16x32_bf16 v[56:59], v[232:235], v[252:255], v[56:59]\n"
            "ds_read_b128 v[0:3], v115 offset:512\n"
            "v_cvt_pk_bf16_f32 v30, v30, v34\n"
            "v_mfma_f32_16x16x32_bf16 v[56:59], v[224:227], v[140:143], v[56:59]\n"
            "global_store_dword v125, v29, s[66:67] offset:0 sc1\n"
            "v_cvt_pk_bf16_f32 v31, v31, v35\n"
            "v_mfma_f32_16x16x32_bf16 v[60:63], v[224:227], v[148:151], v[60:63]\n"
            "v_mfma_f32_16x16x32_bf16 v[60:63], v[232:235], v[152:155], v[60:63]\n"
            "v_mfma_f32_16x16x32_bf16 v[60:63], v[240:243], v[132:135], v[60:63]\n"
            "global_store_dword v126, v30, s[66:67] offset:0 sc1\n"
            "v_mfma_f32_16x16x32_bf16 v[60:63], v[248:251], v[180:183], v[60:63]\n"
            "v_mfma_f32_16x16x32_bf16 v[60:63], v[136:139], v[176:179], v[60:63]\n"
            "v_mfma_f32_16x16x32_bf16 v[60:63], v[144:147], v[184:187], v[60:63]\n"
            "global_store_dword v127, v31, s[66:67] offset:0 sc1\n"
            "s_nop 0\n"
            "v_mfma_f32_16x16x32_bf16 v[88:91], v[212:215], v[184:187], v[88:91]\n"
            "s_nop 1\n"
            "v_mfma_f32_16x16x32_bf16 v[88:91], v[196:199], v[176:179], v[88:91]\n"
            "s_barrier\n"
            "ds_read_b128 v[224:227], v120 offset:28672\n"
            "s_waitcnt lgkmcnt(1)\n"
            "v_mfma_f32_16x16x32_bf16 v[88:91], v[128:131], v[180:183], v[88:91]\n"
            "ds_read_b128 v[232:235], v120 offset:30720\n"
            "v_cvt_pk_bf16_f32 v56, v56, v60\n"
            "v_mfma_f32_16x16x32_bf16 v[88:91], v[188:191], v[132:135], v[88:91]\n"
            "global_store_dword v124, v56, s[68:69] offset:0 sc1\n"
            "v_cvt_pk_bf16_f32 v57, v57, v61\n"
            "v_mfma_f32_16x16x32_bf16 v[88:91], v[172:175], v[152:155], v[88:91]\n"
            "ds_read_b128 v[240:243], v120 offset:32768\n"
            "v_cvt_pk_bf16_f32 v58, v58, v62\n"
            "v_mfma_f32_16x16x32_bf16 v[88:91], v[192:195], v[148:151], v[88:91]\n"
            "global_store_dword v125, v57, s[68:69] offset:0 sc1\n"
            "v_cvt_pk_bf16_f32 v59, v59, v63\n"
            "v_mfma_f32_16x16x32_bf16 v[84:87], v[192:195], v[140:143], v[84:87]\n"
            "ds_read_b128 v[248:251], v120 offset:34816\n"
            "ds_read_b128 v[132:135], v120 offset:36864\n"
            "v_mfma_f32_16x16x32_bf16 v[84:87], v[172:175], v[252:255], v[84:87]\n"
            "ds_read_b128 v[252:255], v120 offset:38912\n"
            "ds_read_b128 v[136:139], v120 offset:51200\n"
            "v_mfma_f32_16x16x32_bf16 v[84:87], v[188:191], v[244:247], v[84:87]\n"
            "global_store_dword v126, v58, s[68:69] offset:0 sc1\n"
            "v_mfma_f32_16x16x32_bf16 v[84:87], v[128:131], v[236:239], v[84:87]\n"
            "ds_read_b128 v[236:239], v120 offset:49152\n"
            "ds_read_b128 v[244:247], v120 offset:47104\n"
            "v_mfma_f32_16x16x32_bf16 v[84:87], v[196:199], v[228:231], v[84:87]\n"
            "ds_read_b128 v[228:231], v120 offset:45056\n"
            "ds_read_b128 v[140:143], v120 offset:43008\n"
            "v_mfma_f32_16x16x32_bf16 v[84:87], v[212:215], v[220:223], v[84:87]\n"
            "global_store_dword v127, v59, s[68:69] offset:0 sc1\n"
            "s_nop 0\n"
            "v_mfma_f32_16x16x32_bf16 v[92:95], v[168:171], v[4:7], v[92:95]\n"
            "ds_read_b128 v[220:223], v120 offset:40960\n"
            "ds_read_b128 v[144:147], v115 offset:768\n"
            "v_mfma_f32_16x16x32_bf16 v[92:95], v[216:219], v[0:3], v[92:95]\n"
            "ds_read_b128 v[148:151], v114 offset:768\n"
            "s_waitcnt lgkmcnt(7)\n"
            "v_mfma_f32_16x16x32_bf16 v[92:95], v[212:215], v[224:227], v[92:95]\n"
            "ds_read_b128 v[152:155], v112 offset:16384\n"
            "v_cvt_pk_bf16_f32 v84, v84, v88\n"
            "v_mfma_f32_16x16x32_bf16 v[92:95], v[196:199], v[232:235], v[92:95]\n"
            "global_store_dword v124, v84, s[70:71] offset:0 sc1\n"
            "v_cvt_pk_bf16_f32 v85, v85, v89\n"
            "v_mfma_f32_16x16x32_bf16 v[92:95], v[128:131], v[240:243], v[92:95]\n"
            "ds_read_b128 v[176:179], v113 offset:16384\n"
            "v_cvt_pk_bf16_f32 v86, v86, v90\n"
            "v_mfma_f32_16x16x32_bf16 v[92:95], v[188:191], v[248:251], v[92:95]\n"
            "global_store_dword v125, v85, s[70:71] offset:0 sc1\n"
            "v_cvt_pk_bf16_f32 v87, v87, v91\n"
            "v_mfma_f32_16x16x32_bf16 v[92:95], v[172:175], v[132:135], v[92:95]\n"
            "ds_read_b128 v[180:183], v118 offset:4096\n"
            "ds_read_b128 v[184:187], v119 offset:4096\n"
            "v_mfma_f32_16x16x32_bf16 v[92:95], v[192:195], v[252:255], v[92:95]\n"
            "ds_read_b128 v[32:35], v118 offset:49152\n"
            "v_mfma_f32_16x16x32_bf16 v[96:99], v[192:195], v[136:139], v[96:99]\n"
            "global_store_dword v126, v86, s[70:71] offset:0 sc1\n"
            "s_waitcnt lgkmcnt(4)\n"
            "v_mfma_f32_16x16x32_bf16 v[96:99], v[172:175], v[236:239], v[96:99]\n"
            "ds_read_b128 v[28:31], v119 offset:49152\n"
            "v_mfma_f32_16x16x32_bf16 v[96:99], v[188:191], v[244:247], v[96:99]\n"
            "ds_read_b128 v[60:63], v112 offset:6144\n"
            "v_mfma_f32_16x16x32_bf16 v[96:99], v[128:131], v[228:231], v[96:99]\n"
            "global_store_dword v127, v87, s[70:71] offset:0 sc1\n"
            "v_mfma_f32_16x16x32_bf16 v[96:99], v[196:199], v[140:143], v[96:99]\n"
            "ds_read_b128 v[56:59], v113 offset:6144\n"
            "v_mfma_f32_16x16x32_bf16 v[96:99], v[212:215], v[220:223], v[96:99]\n"
            "ds_read_b128 v[88:91], v118 offset:2048\n"
            "v_mfma_f32_16x16x32_bf16 v[96:99], v[216:219], v[144:147], v[96:99]\n"
            "ds_read_b128 v[84:87], v119 offset:2048\n"
            "v_mfma_f32_16x16x32_bf16 v[96:99], v[168:171], v[148:151], v[96:99]\n"
            "s_nop 1\n"
            "v_mfma_f32_16x16x32_bf16 v[68:71], v[164:167], v[148:151], v[68:71]\n"
            "s_nop 1\n"
            "v_mfma_f32_16x16x32_bf16 v[68:71], v[208:211], v[144:147], v[68:71]\n"
            "s_nop 0\n"
            "v_mfma_f32_16x16x32_bf16 v[68:71], v[152:155], v[220:223], v[68:71]\n"
            "v_cvt_pk_bf16_f32 v92, v92, v96\n"
            "v_cvt_pk_bf16_f32 v93, v93, v97\n"
            "s_waitcnt lgkmcnt(2)\n"
            "v_mfma_f32_16x16x32_bf16 v[68:71], v[176:179], v[140:143], v[68:71]\n"
            "global_store_dword v124, v92, s[70:71] offset:128 sc1\n"
            "v_cvt_pk_bf16_f32 v94, v94, v98\n"
            "v_mfma_f32_16x16x32_bf16 v[68:71], v[180:183], v[228:231], v[68:71]\n"
            "v_cvt_pk_bf16_f32 v95, v95, v99\n"
            "v_mfma_f32_16x16x32_bf16 v[68:71], v[184:187], v[244:247], v[68:71]\n"
            "global_store_dword v125, v93, s[70:71] offset:128 sc1\n"
            "v_mfma_f32_16x16x32_bf16 v[68:71], v[32:35], v[236:239], v[68:71]\n"
            "ds_read_b128 v[96:99], v118 offset:47104\n"
            "v_mfma_f32_16x16x32_bf16 v[68:71], v[28:31], v[136:139], v[68:71]\n"
            "v_mfma_f32_16x16x32_bf16 v[64:67], v[28:31], v[252:255], v[64:67]\n"
            "global_store_dword v126, v94, s[70:71] offset:128 sc1\n"
            "v_mfma_f32_16x16x32_bf16 v[64:67], v[32:35], v[132:135], v[64:67]\n"
            "v_mfma_f32_16x16x32_bf16 v[64:67], v[184:187], v[248:251], v[64:67]\n"
            "v_mfma_f32_16x16x32_bf16 v[64:67], v[180:183], v[240:243], v[64:67]\n"
            "global_store_dword v127, v95, s[70:71] offset:128 sc1\n"
            "v_mfma_f32_16x16x32_bf16 v[64:67], v[176:179], v[232:235], v[64:67]\n"
            "ds_read_b128 v[92:95], v119 offset:47104\n"
            "v_mfma_f32_16x16x32_bf16 v[64:67], v[152:155], v[224:227], v[64:67]\n"
            "v_mfma_f32_16x16x32_bf16 v[64:67], v[208:211], v[0:3], v[64:67]\n"
            "v_mfma_f32_16x16x32_bf16 v[64:67], v[164:167], v[4:7], v[64:67]\n"
            "s_nop 1\n"
            "v_mfma_f32_16x16x32_bf16 v[36:39], v[160:163], v[4:7], v[36:39]\n"
            "s_nop 1\n"
            "v_mfma_f32_16x16x32_bf16 v[36:39], v[204:207], v[0:3], v[36:39]\n"
            "s_nop 0\n"
            "v_mfma_f32_16x16x32_bf16 v[36:39], v[60:63], v[224:227], v[36:39]\n"
            "v_cvt_pk_bf16_f32 v64, v64, v68\n"
            "v_cvt_pk_bf16_f32 v65, v65, v69\n"
            "v_mfma_f32_16x16x32_bf16 v[36:39], v[56:59], v[232:235], v[36:39]\n"
            "global_store_dword v124, v64, s[68:69] offset:128 sc1\n"
            "v_cvt_pk_bf16_f32 v66, v66, v70\n"
            "s_waitcnt lgkmcnt(0)\n"
            "v_mfma_f32_16x16x32_bf16 v[36:39], v[88:91], v[240:243], v[36:39]\n"
            "v_cvt_pk_bf16_f32 v67, v67, v71\n"
            "v_mfma_f32_16x16x32_bf16 v[36:39], v[84:87], v[248:251], v[36:39]\n"
            "global_store_dword v125, v65, s[68:69] offset:128 sc1\n"
            "v_mfma_f32_16x16x32_bf16 v[36:39], v[96:99], v[132:135], v[36:39]\n"
            "ds_read_b128 v[68:71], v112 offset:4096\n"
            "v_mfma_f32_16x16x32_bf16 v[36:39], v[92:95], v[252:255], v[36:39]\n"
            "v_mfma_f32_16x16x32_bf16 v[40:43], v[92:95], v[136:139], v[40:43]\n"
            "global_store_dword v126, v66, s[68:69] offset:128 sc1\n"
            "v_mfma_f32_16x16x32_bf16 v[40:43], v[96:99], v[236:239], v[40:43]\n"
            "v_mfma_f32_16x16x32_bf16 v[40:43], v[84:87], v[244:247], v[40:43]\n"
            "v_mfma_f32_16x16x32_bf16 v[40:43], v[88:91], v[228:231], v[40:43]\n"
            "global_store_dword v127, v67, s[68:69] offset:128 sc1\n"
            "v_mfma_f32_16x16x32_bf16 v[40:43], v[56:59], v[140:143], v[40:43]\n"
            "ds_read_b128 v[64:67], v113 offset:4096\n"
            "v_mfma_f32_16x16x32_bf16 v[40:43], v[60:63], v[220:223], v[40:43]\n"
            "v_mfma_f32_16x16x32_bf16 v[40:43], v[204:207], v[144:147], v[40:43]\n"
            "v_mfma_f32_16x16x32_bf16 v[40:43], v[160:163], v[148:151], v[40:43]\n"
            "s_nop 1\n"
            "v_mfma_f32_16x16x32_bf16 v[12:15], v[156:159], v[148:151], v[12:15]\n"
            "ds_read_b128 v[148:151], v118 offset:0\n"
            "s_nop 0\n"
            "v_mfma_f32_16x16x32_bf16 v[12:15], v[200:203], v[144:147], v[12:15]\n"
            "s_barrier\n"
            "ds_read_b128 v[144:147], v119 offset:0\n"
            "s_waitcnt lgkmcnt(3)\n"
            "v_mfma_f32_16x16x32_bf16 v[12:15], v[68:71], v[220:223], v[12:15]\n"
            "ds_read_b128 v[220:223], v118 offset:45056\n"
            "v_cvt_pk_bf16_f32 v36, v36, v40\n"
            "s_waitcnt lgkmcnt(3)\n"
            "v_mfma_f32_16x16x32_bf16 v[12:15], v[64:67], v[140:143], v[12:15]\n"
            "ds_read_b128 v[140:143], v119 offset:45056\n"
            "v_cvt_pk_bf16_f32 v37, v37, v41\n"
            "s_waitcnt lgkmcnt(3)\n"
            "v_mfma_f32_16x16x32_bf16 v[12:15], v[148:151], v[228:231], v[12:15]\n"
            "global_store_dword v124, v36, s[66:67] offset:128 sc1\n"
            "v_cvt_pk_bf16_f32 v38, v38, v42\n"
            "s_waitcnt lgkmcnt(2)\n"
            "v_mfma_f32_16x16x32_bf16 v[12:15], v[144:147], v[244:247], v[12:15]\n"
            "ds_read_b128 v[228:231], v114 offset:1536\n"
            "v_cvt_pk_bf16_f32 v39, v39, v43\n"
            "s_waitcnt lgkmcnt(2)\n"
            "v_mfma_f32_16x16x32_bf16 v[12:15], v[220:223], v[236:239], v[12:15]\n"
            "ds_read_b128 v[236:239], v115 offset:1536\n"
            "ds_read_b128 v[244:247], v120 offset:53248\n"
            "s_waitcnt lgkmcnt(3)\n"
            "v_mfma_f32_16x16x32_bf16 v[12:15], v[140:143], v[136:139], v[12:15]\n"
            "global_store_dword v125, v37, s[66:67] offset:128 sc1\n"
            "v_mfma_f32_16x16x32_bf16 v[8:11], v[140:143], v[252:255], v[8:11]\n"
            "ds_read_b128 v[252:255], v120 offset:55296\n"
            "ds_read_b128 v[136:139], v117 offset:8192\n"
            "v_mfma_f32_16x16x32_bf16 v[8:11], v[220:223], v[132:135], v[8:11]\n"
            "global_store_dword v126, v38, s[66:67] offset:128 sc1\n"
            "v_mfma_f32_16x16x32_bf16 v[8:11], v[144:147], v[248:251], v[8:11]\n"
            "ds_read_b128 v[248:251], v117 offset:10240\n"
            "ds_read_b128 v[132:135], v117 offset:20480\n"
            "v_mfma_f32_16x16x32_bf16 v[8:11], v[148:151], v[240:243], v[8:11]\n"
            "ds_read_b128 v[240:243], v117 offset:22528\n"
            "v_mfma_f32_16x16x32_bf16 v[8:11], v[64:67], v[232:235], v[8:11]\n"
            "global_store_dword v127, v39, s[66:67] offset:128 sc1\n"
            "v_mfma_f32_16x16x32_bf16 v[8:11], v[68:71], v[224:227], v[8:11]\n"
            "v_mfma_f32_16x16x32_bf16 v[8:11], v[200:203], v[0:3], v[8:11]\n"
            "v_mfma_f32_16x16x32_bf16 v[8:11], v[156:159], v[4:7], v[8:11]\n"
            "s_nop 1\n"
            "s_waitcnt lgkmcnt(3)\n"
            "v_mfma_f32_16x16x32_bf16 v[24:27], v[228:231], v[156:159], v[24:27]\n"
            "s_nop 1\n"
            "v_mfma_f32_16x16x32_bf16 v[24:27], v[236:239], v[200:203], v[24:27]\n"
            "v_mfma_f32_16x16x32_bf16 v[24:27], v[244:247], v[68:71], v[24:27]\n"
            "v_cvt_pk_bf16_f32 v8, v8, v12\n"
            "v_cvt_pk_bf16_f32 v9, v9, v13\n"
            "v_mfma_f32_16x16x32_bf16 v[24:27], v[252:255], v[64:67], v[24:27]\n"
            "global_store_dword v124, v8, s[64:65] offset:128 sc1\n"
            "v_cvt_pk_bf16_f32 v10, v10, v14\n"
            "v_mfma_f32_16x16x32_bf16 v[24:27], v[136:139], v[148:151], v[24:27]\n"
            "v_cvt_pk_bf16_f32 v11, v11, v15\n"
            "s_waitcnt lgkmcnt(0)\n"
            "v_mfma_f32_16x16x32_bf16 v[24:27], v[248:251], v[144:147], v[24:27]\n"
            "global_store_dword v125, v9, s[64:65] offset:128 sc1\n"
            "v_mfma_f32_16x16x32_bf16 v[24:27], v[132:135], v[220:223], v[24:27]\n"
            "v_mfma_f32_16x16x32_bf16 v[24:27], v[240:243], v[140:143], v[24:27]\n"
            "ds_read_b128 v[224:227], v114 offset:1024\n"
            "s_nop 0\n"
            "v_mfma_f32_16x16x32_bf16 v[52:55], v[240:243], v[92:95], v[52:55]\n"
            "global_store_dword v126, v10, s[64:65] offset:128 sc1\n"
            "s_nop 0\n"
            "v_mfma_f32_16x16x32_bf16 v[52:55], v[132:135], v[96:99], v[52:55]\n"
            "ds_read_b128 v[232:235], v115 offset:1024\n"
            "v_mfma_f32_16x16x32_bf16 v[52:55], v[248:251], v[84:87], v[52:55]\n"
            "ds_read_b128 v[4:7], v117 offset:32768\n"
            "v_cvt_pk_bf16_f32 v24, v24, v25\n"
            "v_mfma_f32_16x16x32_bf16 v[52:55], v[136:139], v[88:91], v[52:55]\n"
            "global_store_dword v127, v11, s[64:65] offset:128 sc1\n"
            "v_cvt_pk_bf16_f32 v25, v26, v27\n"
            "v_mfma_f32_16x16x32_bf16 v[52:55], v[252:255], v[56:59], v[52:55]\n"
            "ds_write_b64 v121, v[24:25] offset:0\n"
            "v_mfma_f32_16x16x32_bf16 v[52:55], v[244:247], v[60:63], v[52:55]\n"
            "ds_read_b128 v[0:3], v117 offset:34816\n"
            "v_mfma_f32_16x16x32_bf16 v[52:55], v[236:239], v[204:207], v[52:55]\n"
            "ds_read_b128 v[40:43], v117 offset:45056\n"
            "v_mfma_f32_16x16x32_bf16 v[52:55], v[228:231], v[160:163], v[52:55]\n"
            "ds_read_b128 v[36:39], v117 offset:47104\n"
            "s_nop 0\n"
            "v_mfma_f32_16x16x32_bf16 v[80:83], v[228:231], v[164:167], v[80:83]\n"
            "ds_read_b128 v[12:15], v117 offset:0\n"
            "s_nop 0\n"
            "v_mfma_f32_16x16x32_bf16 v[80:83], v[236:239], v[208:211], v[80:83]\n"
            "ds_read_b128 v[8:11], v117 offset:2048\n"
            "v_mfma_f32_16x16x32_bf16 v[80:83], v[244:247], v[152:155], v[80:83]\n"
            "ds_read_b128 v[24:27], v117 offset:38912\n"
            "v_cvt_pk_bf16_f32 v52, v52, v53\n"
            "v_mfma_f32_16x16x32_bf16 v[80:83], v[252:255], v[176:179], v[80:83]\n"
            "v_cvt_pk_bf16_f32 v53, v54, v55\n"
            "v_mfma_f32_16x16x32_bf16 v[80:83], v[136:139], v[180:183], v[80:83]\n"
            "ds_write_b64 v121, v[52:53] offset:1024\n"
            "v_mfma_f32_16x16x32_bf16 v[80:83], v[248:251], v[184:187], v[80:83]\n"
            "ds_read_b128 v[52:55], v117 offset:36864\n"
            "v_mfma_f32_16x16x32_bf16 v[80:83], v[132:135], v[32:35], v[80:83]\n"
            "v_mfma_f32_16x16x32_bf16 v[80:83], v[240:243], v[28:31], v[80:83]\n"
            "s_nop 1\n"
            "v_mfma_f32_16x16x32_bf16 v[108:111], v[240:243], v[192:195], v[108:111]\n"
            "ds_read_b128 v[240:243], v117 offset:26624\n"
            "s_nop 0\n"
            "v_mfma_f32_16x16x32_bf16 v[108:111], v[132:135], v[172:175], v[108:111]\n"
            "ds_read_b128 v[132:135], v117 offset:24576\n"
            "v_mfma_f32_16x16x32_bf16 v[108:111], v[248:251], v[188:191], v[108:111]\n"
            "ds_read_b128 v[248:251], v117 offset:14336\n"
            "v_cvt_pk_bf16_f32 v80, v80, v81\n"
            "v_mfma_f32_16x16x32_bf16 v[108:111], v[136:139], v[128:131], v[108:111]\n"
            "s_waitcnt lgkmcnt(3)\n"
            "s_barrier\n"
            "v_mfma_f32_16x16x32_bf16 v[108:111], v[252:255], v[196:199], v[108:111]\n"
            "ds_read_b128 v[252:255], v117 offset:12288\n"
            "v_cvt_pk_bf16_f32 v81, v82, v83\n"
            "v_mfma_f32_16x16x32_bf16 v[108:111], v[244:247], v[212:215], v[108:111]\n"
            "ds_write_b64 v121, v[80:81] offset:2048\n"
            "v_mfma_f32_16x16x32_bf16 v[108:111], v[236:239], v[216:219], v[108:111]\n"
            "ds_read_b128 v[236:239], v115 offset:1280\n"
            "v_mfma_f32_16x16x32_bf16 v[108:111], v[228:231], v[168:171], v[108:111]\n"
            "ds_read_b128 v[228:231], v114 offset:1280\n"
            "s_nop 0\n"
            "v_mfma_f32_16x16x32_bf16 v[16:19], v[156:159], v[224:227], v[16:19]\n"
            "s_nop 1\n"
            "v_mfma_f32_16x16x32_bf16 v[16:19], v[200:203], v[232:235], v[16:19]\n"
            "s_nop 0\n"
            "v_mfma_f32_16x16x32_bf16 v[16:19], v[68:71], v[4:7], v[16:19]\n"
            "v_cvt_pk_bf16_f32 v108, v108, v109\n"
            "v_cvt_pk_bf16_f32 v109, v110, v111\n"
            "v_mfma_f32_16x16x32_bf16 v[16:19], v[64:67], v[0:3], v[16:19]\n"
            "ds_write_b64 v121, v[108:109] offset:3072\n"
            "v_mfma_f32_16x16x32_bf16 v[16:19], v[148:151], v[40:43], v[16:19]\n"
            "v_mfma_f32_16x16x32_bf16 v[16:19], v[144:147], v[36:39], v[16:19]\n"
            "v_mfma_f32_16x16x32_bf16 v[16:19], v[220:223], v[12:15], v[16:19]\n"
            "v_mfma_f32_16x16x32_bf16 v[16:19], v[140:143], v[8:11], v[16:19]\n"
            "v_mfma_f32_16x16x32_bf16 v[20:23], v[140:143], v[24:27], v[20:23]\n"
            "v_mfma_f32_16x16x32_bf16 v[20:23], v[220:223], v[52:55], v[20:23]\n"
            "s_waitcnt lgkmcnt(0)\n"
            "v_mfma_f32_16x16x32_bf16 v[20:23], v[144:147], v[240:243], v[20:23]\n"
            "s_barrier\n"
            "v_mfma_f32_16x16x32_bf16 v[20:23], v[148:151], v[132:135], v[20:23]\n"
            "v_mfma_f32_16x16x32_bf16 v[20:23], v[64:67], v[248:251], v[20:23]\n"
            "v_mfma_f32_16x16x32_bf16 v[20:23], v[68:71], v[252:255], v[20:23]\n"
            "v_mfma_f32_16x16x32_bf16 v[20:23], v[200:203], v[236:239], v[20:23]\n"
            "v_mfma_f32_16x16x32_bf16 v[20:23], v[156:159], v[228:231], v[20:23]\n"
            "s_nop 1\n"
            "v_mfma_f32_16x16x32_bf16 v[48:51], v[160:163], v[228:231], v[48:51]\n"
            "s_nop 1\n"
            "v_mfma_f32_16x16x32_bf16 v[48:51], v[204:207], v[236:239], v[48:51]\n"
            "s_nop 0\n"
            "v_mfma_f32_16x16x32_bf16 v[48:51], v[60:63], v[252:255], v[48:51]\n"
            "v_cvt_pk_bf16_f32 v16, v16, v20\n"
            "v_cvt_pk_bf16_f32 v17, v17, v21\n"
            "v_mfma_f32_16x16x32_bf16 v[48:51], v[56:59], v[248:251], v[48:51]\n"
            "global_store_dword v124, v16, s[64:65] offset:256 sc1\n"
            "v_cvt_pk_bf16_f32 v18, v18, v22\n"
            "v_mfma_f32_16x16x32_bf16 v[48:51], v[88:91], v[132:135], v[48:51]\n"
            "v_cvt_pk_bf16_f32 v19, v19, v23\n"
            "v_mfma_f32_16x16x32_bf16 v[48:51], v[84:87], v[240:243], v[48:51]\n"
            "global_store_dword v125, v17, s[64:65] offset:256 sc1\n"
            "v_mfma_f32_16x16x32_bf16 v[48:51], v[96:99], v[52:55], v[48:51]\n"
            "v_mfma_f32_16x16x32_bf16 v[48:51], v[92:95], v[24:27], v[48:51]\n"
            "v_mfma_f32_16x16x32_bf16 v[44:47], v[92:95], v[8:11], v[44:47]\n"
            "global_store_dword v126, v18, s[64:65] offset:256 sc1\n"
            "v_mfma_f32_16x16x32_bf16 v[44:47], v[96:99], v[12:15], v[44:47]\n"
            "v_mfma_f32_16x16x32_bf16 v[44:47], v[84:87], v[36:39], v[44:47]\n"
            "v_mfma_f32_16x16x32_bf16 v[44:47], v[88:91], v[40:43], v[44:47]\n"
            "global_store_dword v127, v19, s[64:65] offset:256 sc1\n"
            "v_mfma_f32_16x16x32_bf16 v[44:47], v[56:59], v[0:3], v[44:47]\n"
            "v_mfma_f32_16x16x32_bf16 v[44:47], v[60:63], v[4:7], v[44:47]\n"
            "v_mfma_f32_16x16x32_bf16 v[44:47], v[204:207], v[232:235], v[44:47]\n"
            "v_mfma_f32_16x16x32_bf16 v[44:47], v[160:163], v[224:227], v[44:47]\n"
            "s_nop 1\n"
            "v_mfma_f32_16x16x32_bf16 v[72:75], v[164:167], v[224:227], v[72:75]\n"
            "s_nop 1\n"
            "v_mfma_f32_16x16x32_bf16 v[72:75], v[208:211], v[232:235], v[72:75]\n"
            "s_nop 0\n"
            "v_mfma_f32_16x16x32_bf16 v[72:75], v[152:155], v[4:7], v[72:75]\n"
            "v_cvt_pk_bf16_f32 v44, v44, v48\n"
            "v_cvt_pk_bf16_f32 v45, v45, v49\n"
            "v_mfma_f32_16x16x32_bf16 v[72:75], v[176:179], v[0:3], v[72:75]\n"
            "global_store_dword v124, v44, s[66:67] offset:256 sc1\n"
            "v_cvt_pk_bf16_f32 v46, v46, v50\n"
            "v_mfma_f32_16x16x32_bf16 v[72:75], v[180:183], v[40:43], v[72:75]\n"
            "v_cvt_pk_bf16_f32 v47, v47, v51\n"
            "v_mfma_f32_16x16x32_bf16 v[72:75], v[184:187], v[36:39], v[72:75]\n"
            "global_store_dword v125, v45, s[66:67] offset:256 sc1\n"
            "v_mfma_f32_16x16x32_bf16 v[72:75], v[32:35], v[12:15], v[72:75]\n"
            "v_mfma_f32_16x16x32_bf16 v[72:75], v[28:31], v[8:11], v[72:75]\n"
            "v_mfma_f32_16x16x32_bf16 v[76:79], v[28:31], v[24:27], v[76:79]\n"
            "global_store_dword v126, v46, s[66:67] offset:256 sc1\n"
            "v_mfma_f32_16x16x32_bf16 v[76:79], v[32:35], v[52:55], v[76:79]\n"
            "v_mfma_f32_16x16x32_bf16 v[76:79], v[184:187], v[240:243], v[76:79]\n"
            "v_mfma_f32_16x16x32_bf16 v[76:79], v[180:183], v[132:135], v[76:79]\n"
            "global_store_dword v127, v47, s[66:67] offset:256 sc1\n"
            "v_mfma_f32_16x16x32_bf16 v[76:79], v[176:179], v[248:251], v[76:79]\n"
            "v_mfma_f32_16x16x32_bf16 v[76:79], v[152:155], v[252:255], v[76:79]\n"
            "v_mfma_f32_16x16x32_bf16 v[76:79], v[208:211], v[236:239], v[76:79]\n"
            "v_mfma_f32_16x16x32_bf16 v[76:79], v[164:167], v[228:231], v[76:79]\n"
            "s_nop 1\n"
            "v_mfma_f32_16x16x32_bf16 v[104:107], v[168:171], v[228:231], v[104:107]\n"
            "s_nop 1\n"
            "v_mfma_f32_16x16x32_bf16 v[104:107], v[216:219], v[236:239], v[104:107]\n"
            "s_nop 0\n"
            "v_mfma_f32_16x16x32_bf16 v[104:107], v[212:215], v[252:255], v[104:107]\n"
            "v_cvt_pk_bf16_f32 v72, v72, v76\n"
            "v_cvt_pk_bf16_f32 v73, v73, v77\n"
            "v_mfma_f32_16x16x32_bf16 v[104:107], v[196:199], v[248:251], v[104:107]\n"
            "global_store_dword v124, v72, s[68:69] offset:256 sc1\n"
            "v_cvt_pk_bf16_f32 v74, v74, v78\n"
            "v_mfma_f32_16x16x32_bf16 v[104:107], v[128:131], v[132:135], v[104:107]\n"
            "global_store_dword v125, v73, s[68:69] offset:256 sc1\n"
            "v_cvt_pk_bf16_f32 v75, v75, v79\n"
            "v_mfma_f32_16x16x32_bf16 v[104:107], v[188:191], v[240:243], v[104:107]\n"
            "global_store_dword v126, v74, s[68:69] offset:256 sc1\n"
            "v_mfma_f32_16x16x32_bf16 v[104:107], v[172:175], v[52:55], v[104:107]\n"
            "global_store_dword v127, v75, s[68:69] offset:256 sc1\n"
            "v_mfma_f32_16x16x32_bf16 v[104:107], v[192:195], v[24:27], v[104:107]\n"
            "v_mfma_f32_16x16x32_bf16 v[100:103], v[192:195], v[8:11], v[100:103]\n"
            "v_mfma_f32_16x16x32_bf16 v[100:103], v[172:175], v[12:15], v[100:103]\n"
            "v_mfma_f32_16x16x32_bf16 v[100:103], v[188:191], v[36:39], v[100:103]\n"
            "v_mfma_f32_16x16x32_bf16 v[100:103], v[128:131], v[40:43], v[100:103]\n"
            "v_mfma_f32_16x16x32_bf16 v[100:103], v[196:199], v[0:3], v[100:103]\n"
            "v_mfma_f32_16x16x32_bf16 v[100:103], v[212:215], v[4:7], v[100:103]\n"
            "v_mfma_f32_16x16x32_bf16 v[100:103], v[216:219], v[232:235], v[100:103]\n"
            "v_mfma_f32_16x16x32_bf16 v[100:103], v[168:171], v[224:227], v[100:103]\n"
            "s_nop 1\n"
            "s_nop 5\n"
            "v_cvt_pk_bf16_f32 v100, v100, v104\n"
            "v_cvt_pk_bf16_f32 v101, v101, v105\n"
            "v_cvt_pk_bf16_f32 v102, v102, v106\n"
            "v_cvt_pk_bf16_f32 v103, v103, v107\n"
            "global_store_dword v124, v100, s[70:71] offset:256 sc1\n"
            "global_store_dword v125, v101, s[70:71] offset:256 sc1\n"
            "global_store_dword v126, v102, s[70:71] offset:256 sc1\n"
            "global_store_dword v127, v103, s[70:71] offset:256 sc1\n"
            "s_endpgm\n"
            "sq_k_end0:\n"
            :: BODYIN : "memory", "v255", "s55", "s71");
        __builtin_unreachable();
    }
}

extern "C" void kernel_launch(void* const* d_in, const int* in_sizes, int n_in, void* d_out, int out_size,
                              void* d_ws, size_t ws_size, hipStream_t stream) {
    fused_kernel<<<256, 512, 0, stream>>>((const u16*)d_in[0], (const int*)d_in[1],
                                          (const u16*)d_in[2], (u16*)d_out, (char*)d_ws);
}
```
